```python
import torch

def _softmax(x, axis=-1):
    return torch.softmax(x, dim=axis)
def _gelu(x):
    return torch.nn.functional.gelu(x, approximate="tanh")
def _silu(x):
    return torch.nn.functional.silu(x)
def _sigmoid(x):
    return torch.sigmoid(x)
def _layernorm(x, axis=-1, eps=1e-5):
    m = x.mean(dim=axis, keepdim=True)
    v = x.var(dim=axis, keepdim=True, unbiased=False)
    return (x - m) / torch.sqrt(v + eps)
def _scatter_add(tgt, idx, src, axis=0):
    return tgt.clone().index_add_(axis, idx, src)
def _segment_sum(src, seg, n):
    out = torch.zeros((n,) + src.shape[1:], dtype=src.dtype)
    return out.index_add_(0, seg, src)
def _pad(x, pw):
    flat = tuple(v for pair in reversed(pw) for v in pair)
    return torch.nn.functional.pad(x, flat)

kernel_name = 'parallel_sum__sliding_block_cached__swiglu_ffn__decode'

def setup_inputs():

    _w = (lambda a: (lambda t: t.to(torch.bfloat16) if t.is_floating_point() else t)(torch.as_tensor(a)))
    def _A_setup():
        B, Sq, D = 2, 1, 512
        Hq, Hkv, Sk, Dh = 32, 8, 32768, 128
        assert Hq % Hkv == 0, (Hq, Hkv)
        return {
            "x": _w((lambda *s: torch.randn(*s, dtype=torch.bfloat16) * 0.05)(B, Sq, D)),
            "k_cache": _w((lambda *s: torch.randn(*s, dtype=torch.bfloat16) * 3.2)(B, Hkv, Sk, Dh)),
            "v_cache": _w((lambda *s: torch.randn(*s, dtype=torch.bfloat16) * 1.6)(B, Hkv, Sk, Dh)),
            "wq": _w((lambda *s: torch.randn(*s, dtype=torch.bfloat16) * 3.2)(D, Hq * Dh) * (D ** -0.5)),
            "wo": _w((lambda *s: torch.randn(*s, dtype=torch.bfloat16) * 1.6)(Hq * Dh, D) * ((Hq * Dh) ** -0.5)),
        }
    _A_in = _A_setup()

    def _B_setup():
        B, S, D, F = 2, 1, 512, 1792
        return {
            "x": _w((lambda *s: torch.randn(*s, dtype=torch.bfloat16) * 0.05)(B, S, D)),
            "w_gate": _w((lambda *s: torch.randn(*s, dtype=torch.bfloat16) * 0.05)(D, F)),
            "w_up": _w((lambda *s: torch.randn(*s, dtype=torch.bfloat16) * 0.05)(D, F)),
            "w_down": _w((lambda *s: torch.randn(*s, dtype=torch.bfloat16) * 0.05)(F, D)),
        }
    _B_in = _B_setup()

    return {
        "A_x": _A_in["x"],
        "A_k_cache": _A_in["k_cache"],
        "A_v_cache": _A_in["v_cache"],
        "A_wq": _A_in["wq"],
        "A_wo": _A_in["wo"],
        "B_x": _B_in["x"],
        "B_w_gate": _B_in["w_gate"],
        "B_w_up": _B_in["w_up"],
        "B_w_down": _B_in["w_down"],
    }

def reference(A_x, A_k_cache, A_v_cache, A_wq, A_wo, B_x, B_w_gate, B_w_up, B_w_down):
    def _A_ref(x, k_cache, v_cache, wq, wo):
        B, Sq, D = x.shape
        Hq, Dh = 32, 128
        Hkv, Sk = k_cache.shape[1], k_cache.shape[2]
        W = min(4096, max(256, Sk // 4))
        rep = Hq // Hkv
        q = (lambda x, *d: x.permute(*d))((x @ wq).reshape(B, Sq, Hq, Dh), 0, 2, 1, 3)
        k = (lambda x, n, axis: torch.repeat_interleave(x, n, dim=axis))(k_cache[:, :, Sk - W:, :], rep, axis=1)
        v = (lambda x, n, axis: torch.repeat_interleave(x, n, dim=axis))(v_cache[:, :, Sk - W:, :], rep, axis=1)
        scores = torch.einsum("bhqd,bhkd->bhqk", q, k) * (Dh ** -0.5)
        p = _softmax(scores, axis=-1)
        o = torch.einsum("bhqk,bhkd->bhqd", p, v)
        return (lambda x, *d: x.permute(*d))(o, 0, 2, 1, 3).reshape(B, Sq, Hq * Dh) @ wo

    def _B_ref(x, w_gate, w_up, w_down):
        g = _silu(x @ w_gate)
        u = x @ w_up
        return (g * u) @ w_down

    x = A_x
    xn = _layernorm(x, axis=-1)
    return x + _A_ref(xn, A_k_cache, A_v_cache, A_wq, A_wo) + _B_ref(xn, B_w_gate, B_w_up, B_w_down)
```

```cpp
#include <hip/hip_runtime.h>
#include <hip/hip_bf16.h>

#define GAS __attribute__((address_space(1)))
#define LAS __attribute__((address_space(3)))
typedef unsigned v4u __attribute__((ext_vector_type(4)));
typedef unsigned v2u __attribute__((ext_vector_type(2)));
typedef float f4 __attribute__((ext_vector_type(4)));
typedef float f2 __attribute__((ext_vector_type(2)));
typedef short v4s __attribute__((ext_vector_type(4)));
typedef __bf16 v8bf __attribute__((ext_vector_type(8)));
typedef unsigned long long u64;
typedef unsigned short u16;

#define INFL 3
#ifndef NV0
#define NV0 4
#endif
#define PSLEEP 4
#define STAMPS 0

constexpr int NWG = 256, NTHR = 704;
constexpr int D = 512, SK = 32768, DH = 128, WIN = 4096, FF = 1792, NQ = 4096;
constexpr float SCALE = 0.08838834764831845f;
constexpr unsigned SENT = 0xFFFFFFFFu;
constexpr int SPIN_MAX = 1 << 18;

constexpr unsigned WS_E = 0, WS_RR = 4096, WS_QP = 0x40000, WS_S2 = 0x80000, WS_S3 = WS_S2 + 0x100000,
                   WS_GU = WS_S3 + 0x100000, WS_END = WS_GU + 0x80000;
constexpr unsigned WS_DBG = 0x400000;
constexpr int L_K = 0, L_V = 65536, L_W = 131072, L_XN = 147456, L_QS = L_XN + 2048, L_PF = L_QS + 1024,
              L_ML = L_PF + 8192, L_OH = L_ML + 256, L_FLAG = L_OH + 128, L_END = L_FLAG + 64;
constexpr int L_W2 = L_K;
constexpr int L_GU = L_K + 6 * 8192;
enum { F_LANDED = 0, F_PREFILL, F_GEMV, F_Q, F_KREAD, F_PCNT, F_PUB, F_OH, F_PUB3, F_N };
constexpr int C_V0 = 4, C_K = C_V0 + NV0, C_V1 = C_K + 8, C_W2 = 20, C_END = 23;
__host__ __device__ constexpr int vpos(int c) { return c < NV0 ? C_V0 + c : C_V1 + (c - NV0); }

__device__ __forceinline__ float bf_lo(unsigned u) { return __builtin_bit_cast(float, u << 16); }
__device__ __forceinline__ float bf_hi(unsigned u) { return __builtin_bit_cast(float, u & 0xffff0000u); }
__device__ __forceinline__ unsigned f2bf(float f) { return (unsigned)__builtin_bit_cast(u16, (__bf16)f); }
__device__ __forceinline__ unsigned pk2(float lo, float hi) { return f2bf(lo) | (f2bf(hi) << 16); }
__device__ __forceinline__ float rbf(float f) { return (float)(__bf16)f; }
__device__ __forceinline__ unsigned canon(float f) { unsigned u = __builtin_bit_cast(unsigned, f); return (f != f) ? 0x7FC00000u : u; }
template <int CTRL> __device__ __forceinline__ float dpp(float x) {
    return __builtin_bit_cast(float, __builtin_amdgcn_mov_dpp(__builtin_bit_cast(int, x), CTRL, 0xf, 0xf, true));
}
constexpr int XOR1 = 0xB1, XOR2 = 0x4E, XOR7 = 0x141, XOR8 = 0x128, ROR4 = 0x124;
__device__ __forceinline__ float xor4(float x) { return dpp<0x1B>(dpp<XOR7>(x)); }
__device__ __forceinline__ float xsum16(float x) { auto s = __builtin_amdgcn_permlane16_swap(__float_as_uint(x), __float_as_uint(x), false, false); return __uint_as_float(s[0]) + __uint_as_float(s[1]); }
__device__ __forceinline__ float xsum32(float x) { auto s = __builtin_amdgcn_permlane32_swap(__float_as_uint(x), __float_as_uint(x), false, false); return __uint_as_float(s[0]) + __uint_as_float(s[1]); }
__device__ __forceinline__ float xmax16(float x) { auto s = __builtin_amdgcn_permlane16_swap(__float_as_uint(x), __float_as_uint(x), false, false); return fmaxf(__uint_as_float(s[0]), __uint_as_float(s[1])); }
__device__ __forceinline__ float xmax32(float x) { auto s = __builtin_amdgcn_permlane32_swap(__float_as_uint(x), __float_as_uint(x), false, false); return fmaxf(__uint_as_float(s[0]), __uint_as_float(s[1])); }
__device__ __forceinline__ float wave_sum(float x) {
    x += dpp<XOR1>(x); x += dpp<XOR2>(x); x += dpp<XOR7>(x); x += dpp<XOR8>(x); x = xsum16(x); x = xsum32(x); return x;
}
__device__ __forceinline__ f4 mfma(v4u a, v4u b, f4 c) {
    return __builtin_amdgcn_mfma_f32_16x16x32_bf16(__builtin_bit_cast(v8bf, a), __builtin_bit_cast(v8bf, b), c, 0, 0, 0);
}
__device__ __forceinline__ v4u trfrag(LAS unsigned char* a0, LAS unsigned char* a1) {
    const v4s x = __builtin_amdgcn_ds_read_tr16_b64_v4i16((LAS v4s*)a0), y = __builtin_amdgcn_ds_read_tr16_b64_v4i16((LAS v4s*)a1);
    return __builtin_bit_cast(v4u, __builtin_shufflevector(x, y, 0, 1, 2, 3, 4, 5, 6, 7));
}
#define LGKM0() asm volatile("s_waitcnt lgkmcnt(0)" ::: "memory")
#define VM0() asm volatile("s_waitcnt vmcnt(0)" ::: "memory")
#define MEMF() asm volatile("" ::: "memory")
#define BAR() do { __builtin_amdgcn_sched_barrier(0); asm volatile("s_barrier" ::: "memory"); __builtin_amdgcn_sched_barrier(0); } while (0)
#define GLDS(src, dst) __builtin_amdgcn_global_load_lds((const GAS unsigned*)(src), (LAS unsigned*)(dst), 16, 0, 2)
#define ST128(v, off) __builtin_amdgcn_raw_buffer_store_b128((v), rs, (off), 0, 16)
#define LD128(off) __builtin_amdgcn_raw_buffer_load_b128(rs, (off), 0, 16)
#define AST64(p, v) __hip_atomic_store((p), (v), __ATOMIC_RELAXED, __HIP_MEMORY_SCOPE_AGENT)
#define ALD64(p) __hip_atomic_load((p), __ATOMIC_RELAXED, __HIP_MEMORY_SCOPE_AGENT)

__device__ __forceinline__ void wait_flag(volatile LAS unsigned* f, unsigned v) {
    int sp = 0;
#pragma nounroll
    while (*f < v) { __builtin_amdgcn_s_sleep(1); if (++sp > SPIN_MAX) break; }
    MEMF();
}
__device__ __forceinline__ void wait_flag_hot(volatile LAS unsigned* f, unsigned v) {
    int sp = 0;
#pragma nounroll
    while (*f < v) { if (++sp > SPIN_MAX * 8) break; }
    MEMF();
}
__device__ __forceinline__ void flag_inc(int off, int lane) {
    if (lane == 0) asm volatile("ds_add_u32 %0, %1" ::"v"(off), "v"(1u) : "memory");
}
__device__ __forceinline__ v4u gran2(float a, float b, unsigned tl, unsigned th) {
    return v4u{((canon(a) + 0x80u) >> 8) | tl, th, ((canon(b) + 0x80u) >> 8) | tl, th};
}
__device__ __forceinline__ bool gran_ok(v4u v, unsigned tl, unsigned th) { return (((v.x ^ tl) | (v.z ^ tl)) >> 24) == 0u && v.y == th && v.w == th; }
__device__ __forceinline__ float gran_val(unsigned lo) { return __uint_as_float(lo << 8); }
__device__ __forceinline__ void wait_tags(GAS u64* p, u64 tag) {
    int sp = 0;
#pragma nounroll
    for (;;) {
        const u64 v = ALD64(p);
        if (__all(v == tag) || ++sp > SPIN_MAX) break;
        __builtin_amdgcn_s_sleep(PSLEEP);
    }
    MEMF();
}

#define STAMP(k) do {} while (0)
#define STAMP_DUMP() do {} while (0)

__global__ void __launch_bounds__(NTHR, 3)
fused_decode_kernel(const u16* __restrict__ x, const u16* __restrict__ kc, const u16* __restrict__ vc, const u16* __restrict__ wq,
                    const u16* __restrict__ wo, const u16* __restrict__ wg, const u16* __restrict__ wu, const u16* __restrict__ wd,
                    u16* __restrict__ out, unsigned char* ws) {
    __shared__ __attribute__((aligned(16))) unsigned char lds[L_END];
    LAS unsigned char* L8 = (LAS unsigned char*)lds;
    volatile LAS unsigned* FL = (volatile LAS unsigned*)(L8 + L_FLAG);
    const int flo = (int)(unsigned)(size_t)(L8 + L_FLAG);
    const int tid = threadIdx.x, wave = __builtin_amdgcn_readfirstlane(tid >> 6), lane = tid & 63;
    const int wgid = blockIdx.x, g = wgid & 7, i = wgid >> 3, b = i >> 4, s = i & 15;
    const int lg = lane >> 4, lq = (lane >> 2) & 3, lp = lane & 3, l16 = lane & 15;
    const __amdgpu_buffer_rsrc_t rs = __builtin_amdgcn_make_buffer_rsrc(ws, 0, WS_END, 0x00020000);
    const int T = 4 * g + (i >> 3), tr = i & 7;
    const v4u sent4 = {SENT, SENT, SENT, SENT};
    STAMP(0);

    if (wave < 8) {
        const int w = wave;
        if (w == 0) { if (lane < 48) *(LAS unsigned*)(L8 + L_OH + lane * 4) = 0u; LGKM0(); }
        if (lane < 32) ST128(sent4, WS_S2 + (unsigned)(g * 32 + i) * 4096 + w * 512 + lane * 16);
        if (i < 8) { ST128(sent4, WS_S3 + (unsigned)(g * 8 + i) * 16384 + w * 2048 + lane * 16); ST128(sent4, WS_S3 + (unsigned)(g * 8 + i) * 16384 + w * 2048 + 1024 + lane * 16); }
        BAR();
        VM0();
        flag_inc(flo + 4 * F_PREFILL, lane);
        wait_flag(&FL[F_LANDED], C_K + w + 1);
        const int kb = L_K + (32 * w + l16) * 256 + 16 * (lg ^ l16);
        v4u kf[8];
#pragma unroll
        for (int t = 0; t < 4; ++t) { kf[t] = *(const LAS v4u*)(L8 + (kb ^ (64 * t))); kf[4 + t] = *(const LAS v4u*)(L8 + (kb ^ (64 * t)) + 4096); }
        LGKM0();
        if (w < 3) flag_inc(flo + 4 * F_KREAD, lane);
        STAMP(1);
        wait_flag(&FL[F_Q], 1);
        v4u qf[4];
#pragma unroll
        for (int t = 0; t < 4; ++t) qf[t] = *(const LAS v4u*)(L8 + L_QS + lp * 256 + t * 64 + lg * 16);
        STAMP(2);
        f4 sa = {0.f, 0.f, 0.f, 0.f}, sb = {0.f, 0.f, 0.f, 0.f};
#pragma unroll
        for (int t = 0; t < 4; ++t) { sa = mfma(kf[t], qf[t], sa); sb = mfma(kf[4 + t], qf[t], sb); }
        float sc[8];
#pragma unroll
        for (int k = 0; k < 4; ++k) { sc[k] = rbf(rbf(sa[k]) * SCALE); sc[4 + k] = rbf(rbf(sb[k]) * SCALE); }
        float m = fmaxf(fmaxf(fmaxf(sc[0], sc[1]), fmaxf(sc[2], sc[3])), fmaxf(fmaxf(sc[4], sc[5]), fmaxf(sc[6], sc[7])));
        m = xmax16(m); m = xmax32(m);
        float l = 0.f;
#pragma unroll
        for (int k = 0; k < 8; ++k) { sc[k] = __expf(sc[k] - m); l += sc[k]; }
        *(LAS v4u*)(L8 + L_PF + w * 1024 + lane * 16) = v4u{pk2(sc[0], sc[1]), pk2(sc[2], sc[3]), pk2(sc[4], sc[5]), pk2(sc[6], sc[7])};
        l = xsum16(l); l = xsum32(l);
        if (lane < 4) *(LAS f2*)(L8 + L_ML + lane * 64 + w * 8) = f2{m, l};
        flag_inc(flo + 4 * F_PCNT, lane);
        wait_flag_hot(&FL[F_PCNT], 8);
        v4u pf[8]; f4 ml[4];
#pragma unroll
        for (int c = 0; c < 8; ++c) pf[c] = *(const LAS v4u*)(L8 + L_PF + c * 1024 + lane * 16);
#pragma unroll
        for (int c = 0; c < 4; ++c) ml[c] = *(const LAS f4*)(L8 + L_ML + lp * 64 + c * 16);
        STAMP(3);
        float M = fmaxf(fmaxf(fmaxf(ml[0][0], ml[0][2]), fmaxf(ml[1][0], ml[1][2])), fmaxf(fmaxf(ml[2][0], ml[2][2]), fmaxf(ml[3][0], ml[3][2])));
        float al[8], Ls = 0.f;
#pragma unroll
        for (int c = 0; c < 8; ++c) { al[c] = __expf(ml[c >> 1][2 * (c & 1)] - M); Ls = fmaf(al[c], ml[c >> 1][2 * (c & 1) + 1], Ls); }
        const int r7 = (4 * lg + lq) & 7;
        LAS unsigned char* vbp = L8 + L_V + (4 * lg + lq) * 256 + 16 * ((2 * w + (lp >> 1)) ^ (r7 << 1)) + 8 * (lp & 1);
        f4 acc = {0.f, 0.f, 0.f, 0.f};
        const f4 z = {0.f, 0.f, 0.f, 0.f};
#pragma unroll
        for (int c = 0; c < 8; ++c) {
            v4u vf;
            if (c < NV0) vf = trfrag(vbp + c * 8192, vbp + c * 8192 + 4096);
            else {
                int sp = 0;
#pragma nounroll
                for (;;) {
                    const unsigned f = FL[F_LANDED];
                    MEMF();
                    vf = trfrag(vbp + c * 8192, vbp + c * 8192 + 4096);
                    MEMF();
                    if (f >= (unsigned)(vpos(c) + 1) || ++sp > SPIN_MAX * 8) break;
                }
            }
            const f4 d = mfma(vf, pf[c], z);
            acc += al[c] * d;
        }
        STAMP(4);
        const unsigned s2r = WS_S2 + ((unsigned)(g * 32 + lp * 8 + w) * 32 + i) * 128;
        if (l16 < 4) ST128((v4u{canon(acc[0]), canon(acc[1]), canon(acc[2]), canon(acc[3])}), s2r + 16 * lg);
        if (lane >= 4 && lane < 8) __builtin_amdgcn_raw_buffer_store_b64(v2u{canon(M), canon(Ls)}, rs, s2r + 64, 0, 16);
        flag_inc(flo + 4 * F_PUB, lane);
        wait_flag(&FL[F_LANDED], C_END);
        v4u wf[4];
        {
            const int t1 = (lg < 2 && T < 28) ? 16384 : 0;
#pragma unroll
            for (int u = 0; u < 4; ++u) {
                LAS unsigned char* a = L8 + L_W2 + (4 * lg + lq) * 1024 + 16 * ((2 * (4 * w + u) + (lp >> 1)) ^ (r7 << 1)) + 8 * (lp & 1);
                wf[u] = trfrag(a, a + t1);
            }
        }
        STAMP(5);
        v4u ohf;
        {
            int sp = 0;
#pragma nounroll
            for (;;) {
                const unsigned f = FL[F_OH];
                MEMF();
                ohf = *(const LAS v4u*)(L8 + L_OH + (lane & 1) * 64 + lg * 16);
                MEMF();
                if (f >= 2u || ++sp > SPIN_MAX * 8) break;
            }
        }
        const f4 d0 = mfma(wf[0], ohf, z), d1 = mfma(wf[1], ohf, z), d2 = mfma(wf[2], ohf, z), d3 = mfma(wf[3], ohf, z);
        const int us = l16 >> 1;
        const f4 dd = us == 0 ? d0 : us == 1 ? d1 : us == 2 ? d2 : d3;
        if (l16 < 8) ST128((v4u{canon(dd[0]), canon(dd[1]), canon(dd[2]), canon(dd[3])}), WS_S3 + (unsigned)((lane & 1) * 32 + 4 * w + us) * 16384 + wgid * 64 + 16 * lg);
        flag_inc(flo + 4 * F_PUB3, lane);
        STAMP(6);
    } else if (wave == 8) {
#define PACE(c_) do { if ((c_) >= INFL) { asm volatile("s_waitcnt vmcnt(%0)" ::"i"(8 * INFL) : "memory"); FL[F_LANDED] = (c_) - INFL + 1; } } while (0)
        const int wsw = 8 * ((lane & 7) ^ (((lane >> 4) & 3) << 1));
        BAR();
        {
            const u16* src = wq + (size_t)(128 * (i >> 3) + (lane >> 3)) * NQ + 512 * g + 64 * tr + wsw;
#pragma unroll
            for (int j = 0; j < 16; ++j) GLDS(src + (size_t)j * 8 * NQ, L8 + L_W + j * 1024);
        }
        const int Tc = T < 28 ? T : 0;
        {
            const size_t o = (size_t)(64 * tr + (lane >> 3)) * FF + 64 * Tc + wsw;
#pragma unroll
            for (int j = 0; j < 8; ++j) GLDS(wg + o + (size_t)j * 8 * FF, L8 + L_GU + j * 1024);
            PACE(2);
#pragma unroll
            for (int j = 0; j < 8; ++j) GLDS(wu + o + (size_t)j * 8 * FF, L8 + L_GU + 8192 + j * 1024);
            PACE(3);
        }
        const size_t base = ((size_t)(b * 8 + g) * SK + (SK - WIN) + s * 256) * DH + lg * 128;
        const u16* kb = kc + base; const u16* vb = vc + base;
#pragma nounroll
        for (int c = 0; c < 16; ++c) {
            if (c >= NV0 && c < NV0 + 8) {
                const int kcn = c - NV0;
                if (kcn == 6 && FL[F_GEMV] < 1u) { VM0(); FL[F_LANDED] = C_V0 + c; wait_flag(&FL[F_GEMV], 1); }
#pragma unroll
                for (int j = 0; j < 8; ++j) GLDS(kb + kcn * 4096 + j * 512 + 8 * (l16 ^ ((4 * j + lg) & 15)), L8 + L_K + kcn * 8192 + j * 1024);
            } else {
                const int vcn = c < NV0 ? c : c - 8;
#pragma unroll
                for (int j = 0; j < 8; ++j) GLDS(vb + vcn * 4096 + j * 512 + 8 * (l16 ^ (((4 * j + lg) & 7) << 1)), L8 + L_V + vcn * 8192 + j * 1024);
            }
            PACE(C_V0 + c);
        }
        STAMP(1);
        if (FL[F_KREAD] < 3u) { VM0(); FL[F_LANDED] = C_W2; wait_flag(&FL[F_KREAD], 3); }
        {
            const u16* dsrc = wd + (size_t)(64 * Tc + 8 * tr) * D;
#pragma unroll
            for (int j = 0; j < 8; ++j) GLDS(dsrc + j * D + 8 * (lane ^ (j << 1)), L8 + L_W2 + (16 + j) * 1024);
            PACE(C_W2);
            const u16* osrc = wo + (size_t)(512 * g + 16 * i) * D;
#pragma unroll
            for (int j = 0; j < 8; ++j) GLDS(osrc + j * D + 8 * (lane ^ (j << 1)), L8 + L_W2 + j * 1024);
            PACE(C_W2 + 1);
#pragma unroll
            for (int j = 0; j < 8; ++j) GLDS(osrc + (8 + j) * D + 8 * (lane ^ (j << 1)), L8 + L_W2 + (8 + j) * 1024);
            PACE(C_W2 + 2);
        }
        STAMP(2);
#pragma unroll
        for (int k = INFL - 1; k >= 0; --k) {
            asm volatile("s_waitcnt vmcnt(%0)" ::"i"(8 * k) : "memory");
            FL[F_LANDED] = C_END - k;
        }
        STAMP(3);
    } else if (wave == 9) {
        const v4u x0 = *(const GAS v4u*)(x + 8 * lane), x1 = *(const GAS v4u*)(x + D + 8 * lane);
        const u64 tag = ALD64((GAS u64*)(ws + WS_E)) + 1;
        BAR();
#pragma unroll
        for (int bb = 0; bb < 2; ++bb) {
            const v4u xv = bb ? x1 : x0;
            float f[8] = {bf_lo(xv.x), bf_hi(xv.x), bf_lo(xv.y), bf_hi(xv.y), bf_lo(xv.z), bf_hi(xv.z), bf_lo(xv.w), bf_hi(xv.w)};
            float sm = 0.f;
#pragma unroll
            for (int k = 0; k < 8; ++k) sm += f[k];
            const float mean = wave_sum(sm) * (1.f / D);
            float sq = 0.f;
#pragma unroll
            for (int k = 0; k < 8; ++k) { const float d = f[k] - mean; sq += d * d; }
            const float var = rbf(wave_sum(sq) * (1.f / D));
            const float mb = rbf(mean), den = rbf(sqrtf(rbf(var + 1e-5f)));
#pragma unroll
            for (int k = 0; k < 8; ++k) f[k] = rbf(f[k] - mb) / den;
            LAS unsigned char* xp = L8 + L_XN + bb * 1024 + ((lane >> 2) * 32 + 2 * (lane & 1) * 8 + ((lane >> 1) & 1) * 4) * 2;
            *(LAS v2u*)xp = v2u{pk2(f[0], f[1]), pk2(f[2], f[3])};
            *(LAS v2u*)(xp + 16) = v2u{pk2(f[4], f[5]), pk2(f[6], f[7])};
        }
        STAMP(1);
        const unsigned tl = (unsigned)(tag << 24), th = (unsigned)(tag >> 8);
        const int wswz = ((4 * lg + lq) >> 1) & 3;
        const unsigned qpa = WS_QP + ((((unsigned)(g * 2 + (lane & 1)) * 4 + (i >> 3)) * 512) + 64 * tr + 16 * ((l16 >> 1) & 3) + 4 * lg) * 8;
        const unsigned gua = WS_GU + ((((unsigned)(T * 8 + tr) * 2 + (lane & 1)) * 2 + (l16 >> 3)) * 64 + 16 * ((l16 >> 1) & 3) + 4 * lg) * 8;
#pragma nounroll
        for (int pass = 0; pass < 2; ++pass) {
            wait_flag_hot(&FL[F_LANDED], pass ? 4 : 2);
            LAS unsigned char* wa = L8 + (pass ? L_GU : L_W) + (4 * lg + lq) * 128 + 16 * (lp >> 1) + 8 * (lp & 1);
            LAS unsigned char* xa = L8 + L_XN + (lane & 1) * 1024 + lg * 16 + (pass ? 2 * tr : 4 * (i >> 3)) * 64;
            const int xh = pass ? 0 : 128;
            const v4u xf0 = *(const LAS v4u*)xa, xf1 = *(const LAS v4u*)(xa + 64), xf2 = *(const LAS v4u*)(xa + xh), xf3 = *(const LAS v4u*)(xa + xh + 64);
            f4 ac[8];
            const f4 z = {0.f, 0.f, 0.f, 0.f};
#pragma unroll
            for (int nt = 0; nt < 4; ++nt) {
                LAS unsigned char* a = wa + 32 * (nt ^ wswz);
                ac[nt] = mfma(trfrag(a, a + 2048), xf0, z);
                ac[nt] = mfma(trfrag(a + 4096, a + 6144), xf1, ac[nt]);
                ac[4 + nt] = mfma(trfrag(a + 8192, a + 10240), xf2, z);
                ac[4 + nt] = mfma(trfrag(a + 12288, a + 14336), xf3, ac[4 + nt]);
            }
            const int sel = (l16 >> 1) & 3;
            const f4 lo = sel == 0 ? ac[0] : sel == 1 ? ac[1] : sel == 2 ? ac[2] : ac[3];
            const f4 hi = sel == 0 ? ac[4] : sel == 1 ? ac[5] : sel == 2 ? ac[6] : ac[7];
            const f4 r = pass ? ((l16 & 8) ? hi : lo) : lo + hi;
            if (pass == 0) {
                wait_flag_hot(&FL[F_PREFILL], 8);
                if (i < 8 && lane == 8) AST64((GAS u64*)(ws + WS_RR) + g * 8 + i, tag);
            }
            if (pass ? (T < 28) : (l16 < 8)) { const unsigned o = pass ? gua : qpa; ST128(gran2(r[0], r[1], tl, th), o); ST128(gran2(r[2], r[3], tl, th), o + 16); }
            if (pass) { STAMP(3); } else { STAMP(2); }
        }
        FL[F_GEMV] = 1u;
        {
            unsigned qa = WS_QP + (unsigned)(g * 2 + b) * 16384 + lane * 16;
            v4u t[16];
            int sp = 0;
#pragma nounroll
            for (;;) {
                bool ok = true;
                asm volatile("" : "+v"(qa) :: "memory");
#pragma unroll
                for (int j = 0; j < 16; ++j) t[j] = LD128(qa + j * 1024);
#pragma unroll
                for (int j = 0; j < 16; ++j) ok &= gran_ok(t[j], tl, th);
                if (__all(ok) || ++sp > SPIN_MAX) break;
                __builtin_amdgcn_s_sleep(PSLEEP);
            }
#pragma unroll
            for (int h = 0; h < 4; ++h) {
                const float q0 = (gran_val(t[h].x) + gran_val(t[4 + h].x)) + (gran_val(t[8 + h].x) + gran_val(t[12 + h].x));
                const float q1 = (gran_val(t[h].z) + gran_val(t[4 + h].z)) + (gran_val(t[8 + h].z) + gran_val(t[12 + h].z));
                *(LAS unsigned*)(L8 + L_QS + h * 256 + lane * 4) = pk2(q0, q1);
            }
            MEMF();
            FL[F_Q] = 1u;
        }
        STAMP(4);
        if (T < 28) {
            unsigned ga = WS_GU + ((((unsigned)(T * 8 + (lane >> 3)) * 2 + ((lane >> 2) & 1)) * 2 + ((lane >> 1) & 1)) * 64 + 8 * tr + 4 * (lane & 1)) * 8;
            v4u t0, t1;
            int sp = 0;
#pragma nounroll
            for (;;) {
                asm volatile("" : "+v"(ga) :: "memory");
                t0 = LD128(ga); t1 = LD128(ga + 16);
                if (__all(gran_ok(t0, tl, th) && gran_ok(t1, tl, th)) || ++sp > SPIN_MAX) break;
                __builtin_amdgcn_s_sleep(PSLEEP);
            }
            float hv[4] = {gran_val(t0.x), gran_val(t0.z), gran_val(t1.x), gran_val(t1.z)};
#pragma unroll
            for (int k = 0; k < 4; ++k) {
                float v = hv[k];
                v += dpp<XOR8>(v); v = xsum16(v); v = rbf(xsum32(v));
                const float u = dpp<XOR2>(v);
                hv[k] = rbf(v / (1.f + __expf(-v))) * u;
            }
            if (lane < 8 && (lane & 2) == 0) *(LAS v2u*)(L8 + L_OH + ((lane >> 2) & 1) * 64 + ((lane & 1) * 8 + 4) * 2) = v2u{pk2(hv[0], hv[1]), pk2(hv[2], hv[3])};
        }
        wait_tags((GAS u64*)(ws + WS_RR) + lane, tag);
        flag_inc(flo + 4 * F_OH, lane);
        STAMP(5);
    } else {
        BAR();
        wait_flag(&FL[F_PCNT], 8);
        wait_flag_hot(&FL[F_PUB], 8);
        STAMP(1);
        const int p = lane >> 1, half = lane & 1;
        unsigned ra = WS_S2 + ((unsigned)(g * 32 + i) * 32 + p) * 128;
        v4u a0, a1; v2u ml;
        {
            int sp = 0;
#pragma nounroll
            for (;;) {
                asm volatile("" : "+v"(ra) :: "memory");
                a0 = LD128(ra + half * 32);
                a1 = LD128(ra + half * 32 + 16);
                ml = __builtin_amdgcn_raw_buffer_load_b64(rs, ra + 64, 0, 16);
                const bool ok = a0.x != SENT && a0.y != SENT && a0.z != SENT && a0.w != SENT && a1.x != SENT && a1.y != SENT && a1.z != SENT && a1.w != SENT && ml.x != SENT && ml.y != SENT;
                if (__all(ok) || ++sp > SPIN_MAX) break;
            }
        }
        STAMP(2);
        const float m = __uint_as_float(ml.x);
        float M = m;
        M = fmaxf(M, dpp<XOR2>(M)); M = fmaxf(M, dpp<ROR4>(M)); M = fmaxf(M, dpp<XOR8>(M)); M = xmax16(M);
        const float wgt = __expf(m - M);
        float Ls = wgt * __uint_as_float(ml.y);
        Ls += dpp<XOR2>(Ls); Ls += dpp<ROR4>(Ls); Ls += dpp<XOR8>(Ls); Ls = xsum16(Ls);
        float ov[8] = {wgt * __uint_as_float(a0.x), wgt * __uint_as_float(a0.y), wgt * __uint_as_float(a0.z), wgt * __uint_as_float(a0.w),
                       wgt * __uint_as_float(a1.x), wgt * __uint_as_float(a1.y), wgt * __uint_as_float(a1.z), wgt * __uint_as_float(a1.w)};
#pragma unroll
        for (int k = 0; k < 4; ++k) { const float t1 = ov[k] + dpp<XOR8>(ov[k]), t2 = ov[k + 4] + dpp<XOR8>(ov[k + 4]); ov[k] = (lane & 8) ? t2 : t1; }
#pragma unroll
        for (int k = 0; k < 2; ++k) { const float t1 = ov[k] + dpp<XOR2>(ov[k]), t2 = ov[k + 2] + dpp<XOR2>(ov[k + 2]); ov[k] = (lane & 2) ? t2 : t1; }
        { const float t1 = ov[0] + xor4(ov[0]), t2 = ov[1] + xor4(ov[1]); ov[0] = (lane & 4) ? t2 : t1; }
        const float of = xsum16(ov[0]) / Ls;
        const int od = 8 * half + ((lane >> 3) & 1) * 4 + ((lane >> 1) & 1) * 2 + ((lane >> 2) & 1), ob = lane >> 5;
        if ((lane & 16) == 0) *(LAS u16*)(L8 + L_OH + ob * 64 + ((od >> 2) * 8 + (od & 3)) * 2) = (u16)f2bf(of);
        flag_inc(flo + 4 * F_OH, lane);
        STAMP(3);
        if (i < 8) {
            const int rr = g * 8 + i, rb_ = rr >> 5, cb = rr & 31;
            const int q4 = lane & 3;
            const int ocol = 16 * cb + 4 * q4 + ((lane >> 3) & 1) * 2 + ((lane >> 2) & 1);
            const float xres = bf_lo((unsigned)x[rb_ * D + ocol]);
            const u64 E = ALD64((GAS u64*)(ws + WS_E));
            wait_flag_hot(&FL[F_PUB3], 8);
            unsigned sa = WS_S3 + (unsigned)rr * 16384 + lane * 16;
            v4u t[16];
            int sp = 0;
#pragma nounroll
            for (;;) {
                bool ok = true;
                asm volatile("" : "+v"(sa) :: "memory");
#pragma unroll
                for (int j = 0; j < 16; ++j) t[j] = LD128(sa + j * 1024);
#pragma unroll
                for (int j = 0; j < 16; ++j) ok &= t[j].x != SENT && t[j].y != SENT && t[j].z != SENT && t[j].w != SENT;
                if (__all(ok) || ++sp > SPIN_MAX) break;
            }
            STAMP(4);
            float sv[4] = {0.f, 0.f, 0.f, 0.f};
#pragma unroll
            for (int j = 0; j < 16; ++j) { sv[0] += __uint_as_float(t[j].x); sv[1] += __uint_as_float(t[j].y); sv[2] += __uint_as_float(t[j].z); sv[3] += __uint_as_float(t[j].w); }
#pragma unroll
            for (int k = 0; k < 2; ++k) { const float t1 = sv[k] + dpp<XOR8>(sv[k]), t2 = sv[k + 2] + dpp<XOR8>(sv[k + 2]); sv[k] = (lane & 8) ? t2 : t1; }
            { const float t1 = sv[0] + xor4(sv[0]), t2 = sv[1] + xor4(sv[1]); sv[0] = (lane & 4) ? t2 : t1; }
            const float tot = xsum32(xsum16(sv[0]));
            if (lane < 16) out[rb_ * D + ocol] = (u16)f2bf(xres + tot);
            if (wgid == 0 && lane == 0) AST64((GAS u64*)(ws + WS_E), E + 1);
            STAMP(5);
        }
    }
    STAMP_DUMP();
}

extern "C" void kernel_launch(void* const* d_in, const int* in_sizes, int n_in,
                              void* d_out, int out_size, void* d_ws, size_t ws_size,
                              hipStream_t stream) {
    fused_decode_kernel<<<NWG, NTHR, 0, stream>>>((const u16*)d_in[0], (const u16*)d_in[1], (const u16*)d_in[2], (const u16*)d_in[3],
                                                  (const u16*)d_in[4], (const u16*)d_in[6], (const u16*)d_in[7], (const u16*)d_in[8],
                                                  (u16*)d_out, (unsigned char*)d_ws);
}
```

```cpp
#include <hip/hip_runtime.h>
#include <hip/hip_bf16.h>

#define GAS __attribute__((address_space(1)))
#define LAS __attribute__((address_space(3)))
typedef unsigned v4u __attribute__((ext_vector_type(4)));
typedef unsigned v2u __attribute__((ext_vector_type(2)));
typedef float f4 __attribute__((ext_vector_type(4)));
typedef float f2 __attribute__((ext_vector_type(2)));
typedef short v4s __attribute__((ext_vector_type(4)));
typedef __bf16 v8bf __attribute__((ext_vector_type(8)));
typedef unsigned long long u64;
typedef unsigned short u16;

#define INFL 4
#ifndef NV0
#define NV0 4
#endif
#define WAUX 2
#define BARFIRST 1
#define PSLEEP 8
#define ABL 0
#define STAMPS 0

constexpr int NWG = 256, NTHR = 704;
constexpr int D = 512, SK = 32768, DH = 128, WIN = 4096, FF = 1792, NQ = 4096;
constexpr float SCALE = 0.08838834764831845f;
constexpr unsigned SENT = 0xFFFFFFFFu;
constexpr int SPIN_MAX = 1 << 18;

constexpr unsigned WS_E = 0, WS_QP = 0x10000, WS_GU = 0x30000, WS_H = 0x300000, WS_HR = 0x310000, WS_S2 = 0x80000, WS_S3 = WS_S2 + 0x100000, WS_END = 0x340000;
constexpr unsigned WS_DBG = 0x400000;
constexpr int L_K = 0, L_V = 65536, L_W = 131072, L_XN = 147456, L_QS = L_XN + 2048, L_PF = L_QS + 1024,
              L_ML = L_PF + 8192, L_OH = L_ML + 256, L_FLAG = L_OH + 128, L_END = L_FLAG + 64;
constexpr int L_W2 = L_K;
constexpr int L_GU = L_K + 6 * 8192;
enum { F_LANDED = 0, F_PREFILL, F_GEMV, F_Q, F_KREAD, F_PCNT, F_PUB, F_OH, F_PUB3, F_HELLO, F_N };
constexpr int C_V0 = 4, C_K = C_V0 + NV0, C_V1 = C_K + 8, C_W2 = 20, C_END = 23;
__host__ __device__ constexpr int vpos(int c) { return c < NV0 ? C_V0 + c : C_V1 + (c - NV0); }

__device__ __forceinline__ float bf_lo(unsigned u) { return __builtin_bit_cast(float, u << 16); }
__device__ __forceinline__ float bf_hi(unsigned u) { return __builtin_bit_cast(float, u & 0xffff0000u); }
__device__ __forceinline__ unsigned f2bf(float f) { return (unsigned)__builtin_bit_cast(u16, (__bf16)f); }
__device__ __forceinline__ unsigned pk2(float lo, float hi) { return f2bf(lo) | (f2bf(hi) << 16); }
__device__ __forceinline__ float rbf(float f) { return (float)(__bf16)f; }
__device__ __forceinline__ unsigned canon(float f) { unsigned u = __builtin_bit_cast(unsigned, f); return (f != f) ? 0x7FC00000u : u; }
template <int CTRL> __device__ __forceinline__ float dpp(float x) {
    return __builtin_bit_cast(float, __builtin_amdgcn_mov_dpp(__builtin_bit_cast(int, x), CTRL, 0xf, 0xf, true));
}
constexpr int XOR1 = 0xB1, XOR2 = 0x4E, XOR7 = 0x141, XOR8 = 0x128, ROR4 = 0x124;
__device__ __forceinline__ float xor4(float x) { return dpp<0x1B>(dpp<XOR7>(x)); }
__device__ __forceinline__ float xsum16(float x) { auto s = __builtin_amdgcn_permlane16_swap(__float_as_uint(x), __float_as_uint(x), false, false); return __uint_as_float(s[0]) + __uint_as_float(s[1]); }
__device__ __forceinline__ float xsum32(float x) { auto s = __builtin_amdgcn_permlane32_swap(__float_as_uint(x), __float_as_uint(x), false, false); return __uint_as_float(s[0]) + __uint_as_float(s[1]); }
__device__ __forceinline__ float xmax16(float x) { auto s = __builtin_amdgcn_permlane16_swap(__float_as_uint(x), __float_as_uint(x), false, false); return fmaxf(__uint_as_float(s[0]), __uint_as_float(s[1])); }
__device__ __forceinline__ float xmax32(float x) { auto s = __builtin_amdgcn_permlane32_swap(__float_as_uint(x), __float_as_uint(x), false, false); return fmaxf(__uint_as_float(s[0]), __uint_as_float(s[1])); }
__device__ __forceinline__ float wave_sum(float x) {
    x += dpp<XOR1>(x); x += dpp<XOR2>(x); x += dpp<XOR7>(x); x += dpp<XOR8>(x); x = xsum16(x); x = xsum32(x); return x;
}
__device__ __forceinline__ f4 mfma(v4u a, v4u b, f4 c) {
    return __builtin_amdgcn_mfma_f32_16x16x32_bf16(__builtin_bit_cast(v8bf, a), __builtin_bit_cast(v8bf, b), c, 0, 0, 0);
}
__device__ __forceinline__ v4u trfrag(LAS unsigned char* a0, LAS unsigned char* a1) {
    const v4s x = __builtin_amdgcn_ds_read_tr16_b64_v4i16((LAS v4s*)a0), y = __builtin_amdgcn_ds_read_tr16_b64_v4i16((LAS v4s*)a1);
    return __builtin_bit_cast(v4u, __builtin_shufflevector(x, y, 0, 1, 2, 3, 4, 5, 6, 7));
}
#define LGKM0() asm volatile("s_waitcnt lgkmcnt(0)" ::: "memory")
#define VM0() asm volatile("s_waitcnt vmcnt(0)" ::: "memory")
#define MEMF() asm volatile("" ::: "memory")
#define BAR() do { __builtin_amdgcn_sched_barrier(0); asm volatile("s_barrier" ::: "memory"); __builtin_amdgcn_sched_barrier(0); } while (0)
#define GLDS(src, dst) __builtin_amdgcn_global_load_lds((const GAS unsigned*)(src), (LAS unsigned*)(dst), 16, 0, 2)
#define GLDSW(src, dst) __builtin_amdgcn_global_load_lds((const GAS unsigned*)(src), (LAS unsigned*)(dst), 16, 0, WAUX)

__device__ __forceinline__ void wait_flag(volatile LAS unsigned* f, unsigned v) {
    int sp = 0;
#pragma nounroll
    while (*f < v) { __builtin_amdgcn_s_sleep(1); if (++sp > SPIN_MAX) break; }
    MEMF();
}
__device__ __forceinline__ void wait_flag_hot(volatile LAS unsigned* f, unsigned v) {
    int sp = 0;
#pragma nounroll
    while (*f < v) { if (++sp > SPIN_MAX * 8) break; }
    MEMF();
}
__device__ __forceinline__ void flag_inc(int off, int lane) {
    if (lane == 0) asm volatile("ds_add_u32 %0, %1" ::"v"(off), "v"(1u) : "memory");
}

#define STAMP(k) do {} while (0)
#define STAMP_DUMP() do {} while (0)

__global__ void __launch_bounds__(NTHR, 3)
fused_decode_kernel(const u16* __restrict__ x, const u16* __restrict__ kc, const u16* __restrict__ vc, const u16* __restrict__ wq,
                    const u16* __restrict__ wo, const u16* __restrict__ wg, const u16* __restrict__ wu, const u16* __restrict__ wd,
                    u16* __restrict__ out, unsigned char* ws) {
    __shared__ __attribute__((aligned(16))) unsigned char lds[L_END];
    LAS unsigned char* L8 = (LAS unsigned char*)lds;
    volatile LAS unsigned* FL = (volatile LAS unsigned*)(L8 + L_FLAG);
    const int flo = (int)(unsigned)(size_t)(L8 + L_FLAG);
    const int tid = threadIdx.x, wave = __builtin_amdgcn_readfirstlane(tid >> 6), lane = tid & 63;
    const int wgid = blockIdx.x, g = wgid & 7, i = wgid >> 3, b = i >> 4, s = i & 15;
    const int lg = lane >> 4, lq = (lane >> 2) & 3, lp = lane & 3, l16 = lane & 15;
    const __amdgpu_buffer_rsrc_t rs = __builtin_amdgcn_make_buffer_rsrc(ws, 0, WS_END, 0x00020000);
    const int T = 4 * g + (i >> 3), tr = i & 7;
    const unsigned qpa = WS_QP + ((((unsigned)(g * 2 + (lane & 1)) * 4 + (i >> 3)) * 512) + 64 * tr + 16 * ((l16 >> 1) & 3) + 4 * lg) * 4;
    const unsigned gua = WS_GU + ((((unsigned)(T * 8 + tr) * 2 + (lane & 1)) * 2 + (l16 >> 3)) * 64 + 16 * ((l16 >> 1) & 3) + 4 * lg) * 4;
    STAMP(0);

    if (wave < 8) {
        const int w = wave;
        if (w == 0) { if (lane < 48) *(LAS unsigned*)(L8 + L_OH + lane * 4) = 0u; LGKM0(); }
        const unsigned s2a = WS_S2 + (((unsigned)(g * 32 + lp * 8 + w) * 32 + i) * 32 + 4 * lg) * 4;
        const unsigned s2b = WS_S2 + (((unsigned)(g * 32 + lp * 8 + w) * 32 + i) * 32 + 16) * 4;
        const unsigned s3a = WS_S3 + (unsigned)wgid * 4096 + ((lane & 1) * 512 + 16 * (4 * w + (l16 >> 1)) + 4 * lg) * 4;
        if (!(ABL & 1)) {
        if (l16 < 4) __builtin_amdgcn_raw_buffer_store_b128(v4u{SENT, SENT, SENT, SENT}, rs, s2a, 0, 16);
        if (lane >= 4 && lane < 8) __builtin_amdgcn_raw_buffer_store_b64(v2u{SENT, SENT}, rs, s2b, 0, 16);
        if (l16 < 8) __builtin_amdgcn_raw_buffer_store_b128(v4u{SENT, SENT, SENT, SENT}, rs, s3a, 0, 16);
        }
        if (w == 1 && l16 < 8) __builtin_amdgcn_raw_buffer_store_b128(v4u{SENT, SENT, SENT, SENT}, rs, qpa, 0, 16);
        if (w == 2) __builtin_amdgcn_raw_buffer_store_b128(v4u{SENT, SENT, SENT, SENT}, rs, gua, 0, 16);
        BAR();
        VM0();
        flag_inc(flo + 4 * F_PREFILL, lane);
        wait_flag(&FL[F_LANDED], C_K + w + 1);
        const int kb = L_K + (32 * w + l16) * 256 + 16 * (lg ^ l16);
        v4u kf[8];
#pragma unroll
        for (int t = 0; t < 4; ++t) { kf[t] = *(const LAS v4u*)(L8 + (kb ^ (64 * t))); kf[4 + t] = *(const LAS v4u*)(L8 + (kb ^ (64 * t)) + 4096); }
        LGKM0();
        if (w < 3) flag_inc(flo + 4 * F_KREAD, lane);
        STAMP(1);
        if (ABL & 8) { STAMP_DUMP(); return; }
        wait_flag(&FL[F_Q], 1);
        v4u qf[4];
        {
            int sp = 0;
#pragma nounroll
            for (;;) {
                const unsigned f = FL[F_Q];
                MEMF();
#pragma unroll
                for (int t = 0; t < 4; ++t) qf[t] = *(const LAS v4u*)(L8 + L_QS + lp * 256 + t * 64 + lg * 16);
                MEMF();
                if (f != 0u || ++sp > SPIN_MAX * 8) break;
            }
        }
        STAMP(2);
        f4 sa = {0.f, 0.f, 0.f, 0.f}, sb = {0.f, 0.f, 0.f, 0.f};
#pragma unroll
        for (int t = 0; t < 4; ++t) { sa = mfma(kf[t], qf[t], sa); sb = mfma(kf[4 + t], qf[t], sb); }
        float sc[8];
#pragma unroll
        for (int k = 0; k < 4; ++k) { sc[k] = rbf(rbf(sa[k]) * SCALE); sc[4 + k] = rbf(rbf(sb[k]) * SCALE); }
        float m = fmaxf(fmaxf(fmaxf(sc[0], sc[1]), fmaxf(sc[2], sc[3])), fmaxf(fmaxf(sc[4], sc[5]), fmaxf(sc[6], sc[7])));
        m = xmax16(m); m = xmax32(m);
        float l = 0.f;
#pragma unroll
        for (int k = 0; k < 8; ++k) { sc[k] = __expf(sc[k] - m); l += sc[k]; }
        *(LAS v4u*)(L8 + L_PF + w * 1024 + lane * 16) = v4u{pk2(sc[0], sc[1]), pk2(sc[2], sc[3]), pk2(sc[4], sc[5]), pk2(sc[6], sc[7])};
        l = xsum16(l); l = xsum32(l);
        if (lane < 4) *(LAS f2*)(L8 + L_ML + lane * 64 + w * 8) = f2{m, l};
        flag_inc(flo + 4 * F_PCNT, lane);
        v4u pf[8]; f4 ml[4];
        {
            int sp = 0;
#pragma nounroll
            for (;;) {
                const unsigned f = FL[F_PCNT];
                MEMF();
#pragma unroll
                for (int c = 0; c < 8; ++c) pf[c] = *(const LAS v4u*)(L8 + L_PF + c * 1024 + lane * 16);
#pragma unroll
                for (int c = 0; c < 4; ++c) ml[c] = *(const LAS f4*)(L8 + L_ML + lp * 64 + c * 16);
                MEMF();
                if (f >= 8u || ++sp > SPIN_MAX * 8) break;
            }
        }
        STAMP(3);
        float M = fmaxf(fmaxf(fmaxf(ml[0][0], ml[0][2]), fmaxf(ml[1][0], ml[1][2])), fmaxf(fmaxf(ml[2][0], ml[2][2]), fmaxf(ml[3][0], ml[3][2])));
        float al[8], Ls = 0.f;
#pragma unroll
        for (int c = 0; c < 8; ++c) { al[c] = __expf(ml[c >> 1][2 * (c & 1)] - M); Ls = fmaf(al[c], ml[c >> 1][2 * (c & 1) + 1], Ls); }
        const int r7 = (4 * lg + lq) & 7;
        LAS unsigned char* vbp = L8 + L_V + (4 * lg + lq) * 256 + 16 * ((2 * w + (lp >> 1)) ^ (r7 << 1)) + 8 * (lp & 1);
        f4 acc = {0.f, 0.f, 0.f, 0.f};
        const f4 z = {0.f, 0.f, 0.f, 0.f};
#pragma unroll
        for (int c = 0; c < 8; ++c) {
            v4u vf;
            if (c < NV0) vf = trfrag(vbp + c * 8192, vbp + c * 8192 + 4096);
            else {
                int sp = 0;
#pragma nounroll
                for (;;) {
                    const unsigned f = FL[F_LANDED];
                    MEMF();
                    vf = trfrag(vbp + c * 8192, vbp + c * 8192 + 4096);
                    MEMF();
                    if (f >= (unsigned)(vpos(c) + 1) || ++sp > SPIN_MAX * 8) break;
                }
            }
            const f4 d = mfma(vf, pf[c], z);
            acc += al[c] * d;
        }
        STAMP(4);
        if (l16 < 4) __builtin_amdgcn_raw_buffer_store_b128(v4u{canon(acc[0]), canon(acc[1]), canon(acc[2]), canon(acc[3])}, rs, s2a, 0, 16);
        if (lane >= 4 && lane < 8) __builtin_amdgcn_raw_buffer_store_b64(v2u{canon(M), canon(Ls)}, rs, s2b, 0, 16);
        flag_inc(flo + 4 * F_PUB, lane);
        wait_flag(&FL[F_LANDED], C_END);
        v4u wf[4];
        {
            const int t1 = (lg < 2 && T < 28) ? 16384 : 0;
#pragma unroll
            for (int u = 0; u < 4; ++u) {
                LAS unsigned char* a = L8 + L_W2 + (4 * lg + lq) * 1024 + 16 * ((2 * (4 * w + u) + (lp >> 1)) ^ (r7 << 1)) + 8 * (lp & 1);
                wf[u] = trfrag(a, a + t1);
            }
        }
        STAMP(5);
        v4u ohf;
        {
            int sp = 0;
#pragma nounroll
            for (;;) {
                const unsigned f = FL[F_OH];
                MEMF();
                ohf = *(const LAS v4u*)(L8 + L_OH + (lane & 1) * 64 + lg * 16);
                MEMF();
                if (f >= 2u || ++sp > SPIN_MAX * 8) break;
            }
        }
        const f4 d0 = mfma(wf[0], ohf, z), d1 = mfma(wf[1], ohf, z), d2 = mfma(wf[2], ohf, z), d3 = mfma(wf[3], ohf, z);
        const int us = l16 >> 1;
        const f4 dd = us == 0 ? d0 : us == 1 ? d1 : us == 2 ? d2 : d3;
        if (l16 < 8) __builtin_amdgcn_raw_buffer_store_b128(v4u{canon(dd[0]), canon(dd[1]), canon(dd[2]), canon(dd[3])}, rs, s3a, 0, 16);
        flag_inc(flo + 4 * F_PUB3, lane);
        STAMP(6);
    } else if (wave == 8) {
#define PACE(c_) do { if ((c_) >= INFL) { asm volatile("s_waitcnt vmcnt(%0)" ::"i"(8 * INFL) : "memory"); FL[F_LANDED] = (c_) - INFL + 1; } } while (0)
        const int wsw = 8 * ((lane & 7) ^ (((lane >> 4) & 3) << 1));
        BAR();
        {
            const u16* src = wq + (size_t)(128 * (i >> 3) + (lane >> 3)) * NQ + 512 * g + 64 * tr + wsw;
#pragma unroll
            for (int j = 0; j < 16; ++j) GLDS(src + (size_t)j * 8 * NQ, L8 + L_W + j * 1024);
        }
        const int Tc = T < 28 ? T : 0;
        {
            const size_t o = (size_t)(64 * tr + (lane >> 3)) * FF + 64 * Tc + wsw;
#pragma unroll
            for (int j = 0; j < 8; ++j) GLDS(wg + o + (size_t)j * 8 * FF, L8 + L_GU + j * 1024);
            PACE(2);
#pragma unroll
            for (int j = 0; j < 8; ++j) GLDS(wu + o + (size_t)j * 8 * FF, L8 + L_GU + 8192 + j * 1024);
            PACE(3);
        }
        const size_t base = ((size_t)(b * 8 + g) * SK + (SK - WIN) + s * 256) * DH + lg * 128;
        const u16* kb = kc + base; const u16* vb = vc + base;
#pragma nounroll
        for (int c = 0; c < 16; ++c) {
            if (c >= NV0 && c < NV0 + 8) {
                const int kcn = c - NV0;
                if (kcn == 6 && FL[F_GEMV] < 1u) { VM0(); FL[F_LANDED] = C_V0 + c; wait_flag(&FL[F_GEMV], 1); }
#pragma unroll
                for (int j = 0; j < 8; ++j) GLDS(kb + kcn * 4096 + j * 512 + 8 * (l16 ^ ((4 * j + lg) & 15)), L8 + L_K + kcn * 8192 + j * 1024);
            } else {
                const int vcn = c < NV0 ? c : c - 8;
#pragma unroll
                for (int j = 0; j < 8; ++j) GLDS(vb + vcn * 4096 + j * 512 + 8 * (l16 ^ (((4 * j + lg) & 7) << 1)), L8 + L_V + vcn * 8192 + j * 1024);
            }
            PACE(C_V0 + c);
        }
        STAMP(1);
        if (FL[F_KREAD] < 3u) { VM0(); FL[F_LANDED] = C_W2; wait_flag(&FL[F_KREAD], 3); }
        {
            const u16* dsrc = wd + (size_t)(64 * Tc + 8 * tr) * D;
#pragma unroll
            for (int j = 0; j < 8; ++j) GLDS(dsrc + j * D + 8 * (lane ^ (j << 1)), L8 + L_W2 + (16 + j) * 1024);
            PACE(C_W2);
            const u16* osrc = wo + (size_t)(512 * g + 16 * i) * D;
#pragma unroll
            for (int j = 0; j < 8; ++j) GLDS(osrc + j * D + 8 * (lane ^ (j << 1)), L8 + L_W2 + j * 1024);
            PACE(C_W2 + 1);
#pragma unroll
            for (int j = 0; j < 8; ++j) GLDS(osrc + (8 + j) * D + 8 * (lane ^ (j << 1)), L8 + L_W2 + (8 + j) * 1024);
            PACE(C_W2 + 2);
        }
        STAMP(2);
#pragma unroll
        for (int k = INFL - 1; k >= 0; --k) {
            asm volatile("s_waitcnt vmcnt(%0)" ::"i"(8 * k) : "memory");
            FL[F_LANDED] = C_END - k;
        }
        STAMP(3);
    } else if (wave == 9) {
        const v4u x0 = *(const GAS v4u*)(x + 8 * lane), x1 = *(const GAS v4u*)(x + D + 8 * lane);
        BAR();
#pragma unroll
        for (int bb = 0; bb < 2; ++bb) {
            const v4u xv = bb ? x1 : x0;
            float f[8] = {bf_lo(xv.x), bf_hi(xv.x), bf_lo(xv.y), bf_hi(xv.y), bf_lo(xv.z), bf_hi(xv.z), bf_lo(xv.w), bf_hi(xv.w)};
            float sm = 0.f;
#pragma unroll
            for (int k = 0; k < 8; ++k) sm += f[k];
            const float mean = wave_sum(sm) * (1.f / D);
            float sq = 0.f;
#pragma unroll
            for (int k = 0; k < 8; ++k) { const float d = f[k] - mean; sq += d * d; }
            const float var = rbf(wave_sum(sq) * (1.f / D));
            const float mb = rbf(mean), den = rbf(sqrtf(rbf(var + 1e-5f)));
#pragma unroll
            for (int k = 0; k < 8; ++k) f[k] = rbf(f[k] - mb) / den;
            LAS unsigned char* xp = L8 + L_XN + bb * 1024 + ((lane >> 2) * 32 + 2 * (lane & 1) * 8 + ((lane >> 1) & 1) * 4) * 2;
            *(LAS v2u*)xp = v2u{pk2(f[0], f[1]), pk2(f[2], f[3])};
            *(LAS v2u*)(xp + 16) = v2u{pk2(f[4], f[5]), pk2(f[6], f[7])};
        }
        STAMP(1);
        const int wswz = ((4 * lg + lq) >> 1) & 3;
#pragma nounroll
        for (int pass = 0; pass < 2; ++pass) {
            wait_flag_hot(&FL[F_LANDED], pass ? 4 : 2);
            LAS unsigned char* wa = L8 + (pass ? L_GU : L_W) + (4 * lg + lq) * 128 + 16 * (lp >> 1) + 8 * (lp & 1);
            LAS unsigned char* xa = L8 + L_XN + (lane & 1) * 1024 + lg * 16 + (pass ? 2 * tr : 4 * (i >> 3)) * 64;
            const int xh = pass ? 0 : 128;
            const v4u xf0 = *(const LAS v4u*)xa, xf1 = *(const LAS v4u*)(xa + 64), xf2 = *(const LAS v4u*)(xa + xh), xf3 = *(const LAS v4u*)(xa + xh + 64);
            f4 ac[8];
            const f4 z = {0.f, 0.f, 0.f, 0.f};
#pragma unroll
            for (int nt = 0; nt < 4; ++nt) {
                LAS unsigned char* a = wa + 32 * (nt ^ wswz);
                ac[nt] = mfma(trfrag(a, a + 2048), xf0, z);
                ac[nt] = mfma(trfrag(a + 4096, a + 6144), xf1, ac[nt]);
                ac[4 + nt] = mfma(trfrag(a + 8192, a + 10240), xf2, z);
                ac[4 + nt] = mfma(trfrag(a + 12288, a + 14336), xf3, ac[4 + nt]);
            }
            const int sel = (l16 >> 1) & 3;
            const f4 lo = sel == 0 ? ac[0] : sel == 1 ? ac[1] : sel == 2 ? ac[2] : ac[3];
            const f4 hi = sel == 0 ? ac[4] : sel == 1 ? ac[5] : sel == 2 ? ac[6] : ac[7];
            const f4 r = pass ? ((l16 & 8) ? hi : lo) : lo + hi;
            if (pass == 0) wait_flag_hot(&FL[F_PREFILL], 8);
            if (pass ? (T < 28) : (l16 < 8)) __builtin_amdgcn_raw_buffer_store_b128(v4u{canon(r[0]), canon(r[1]), canon(r[2]), canon(r[3])}, rs, pass ? gua : qpa, 0, 16);
            if (pass) { STAMP(3); } else { STAMP(2); }
        }
        FL[F_GEMV] = 1u;
        if (ABL & 4) { STAMP_DUMP(); return; }
        wait_flag(&FL[F_HELLO], 1);
        {
            unsigned qa = WS_QP + (unsigned)(g * 2 + b) * 8192 + lane * 16;
            v4u t[8];
            int sp = 0;
#pragma nounroll
            for (;;) {
                bool ok = true;
                asm volatile("" : "+v"(qa) :: "memory");
#pragma unroll
                for (int j = 0; j < 8; ++j) t[j] = __builtin_amdgcn_raw_buffer_load_b128(rs, qa + j * 1024, 0, 16);
#pragma unroll
                for (int j = 0; j < 8; ++j) ok &= t[j].x != SENT && t[j].y != SENT && t[j].z != SENT && t[j].w != SENT;
                if (__all(ok) || ++sp > SPIN_MAX) break;
                __builtin_amdgcn_s_sleep(PSLEEP);
            }
#pragma unroll
            for (int h = 0; h < 2; ++h) {
                const f4 q4 = (__builtin_bit_cast(f4, t[h]) + __builtin_bit_cast(f4, t[2 + h])) + (__builtin_bit_cast(f4, t[4 + h]) + __builtin_bit_cast(f4, t[6 + h]));
                *(LAS v2u*)(L8 + L_QS + h * 512 + lane * 8) = v2u{pk2(q4[0], q4[1]), pk2(q4[2], q4[3])};
            }
            MEMF();
            FL[F_Q] = 1u;
        }
        STAMP(4);
        if (T < 28) {
            unsigned ga = WS_GU + ((((unsigned)(T * 8 + (lane >> 3)) * 2 + ((lane >> 2) & 1)) * 2 + ((lane >> 1) & 1)) * 64 + 8 * tr + 4 * (lane & 1)) * 4;
            v4u t;
            int sp = 0;
#pragma nounroll
            for (;;) {
                asm volatile("" : "+v"(ga) :: "memory");
                t = __builtin_amdgcn_raw_buffer_load_b128(rs, ga, 0, 16);
                const bool ok = t.x != SENT && t.y != SENT && t.z != SENT && t.w != SENT;
                if (__all(ok) || ++sp > SPIN_MAX) break;
                __builtin_amdgcn_s_sleep(PSLEEP);
            }
            unsigned hp[2];
            float hv[4];
#pragma unroll
            for (int k = 0; k < 4; ++k) {
                float v = __uint_as_float(t[k]);
                v += dpp<XOR8>(v); v = xsum16(v); v = rbf(xsum32(v));
                const float u = dpp<XOR2>(v);
                hv[k] = rbf(v / (1.f + __expf(-v))) * u;
            }
            hp[0] = pk2(hv[0], hv[1]); hp[1] = pk2(hv[2], hv[3]);
            if (lane < 8 && (lane & 2) == 0) *(LAS v2u*)(L8 + L_OH + ((lane >> 2) & 1) * 64 + ((lane & 1) * 8 + 4) * 2) = v2u{hp[0], hp[1]};
        }
        flag_inc(flo + 4 * F_OH, lane);
        STAMP(5);
    } else {
        const u64 E = __hip_atomic_load((GAS u64*)(ws + WS_E), __ATOMIC_RELAXED, __HIP_MEMORY_SCOPE_AGENT);
        BAR();
        if (ABL & 2) { STAMP_DUMP(); return; }
        {
            GAS u64* hg = (GAS u64*)(ws + WS_H);
            GAS u64* hr = (GAS u64*)(ws + WS_HR);
            wait_flag_hot(&FL[F_PREFILL], 8);
            if (lane < 32) __hip_atomic_store(hg + (g * 32 + lane) * 32 + i, E + 1, __ATOMIC_RELAXED, __HIP_MEMORY_SCOPE_AGENT);
            __hip_atomic_store(hr + lane * 256 + wgid, E + 1, __ATOMIC_RELAXED, __HIP_MEMORY_SCOPE_AGENT);
            int sp = 0;
#pragma nounroll
            for (;;) {
                __builtin_amdgcn_s_sleep(PSLEEP);
                const u64 v = __hip_atomic_load(hg + (g * 32 + i) * 32 + (lane & 31), __ATOMIC_RELAXED, __HIP_MEMORY_SCOPE_AGENT);
                if (__all(v == E + 1) || ++sp > SPIN_MAX) break;
            }
            FL[F_HELLO] = 1u;
            STAMP(6);
            if (i < 8) {
                sp = 0;
#pragma nounroll
                for (;;) {
                    bool ok = true;
#pragma unroll
                    for (int j = 0; j < 4; ++j) ok &= __hip_atomic_load(hr + (g * 8 + i) * 256 + 64 * j + lane, __ATOMIC_RELAXED, __HIP_MEMORY_SCOPE_AGENT) == E + 1;
                    if (__all(ok) || ++sp > SPIN_MAX) break;
                    __builtin_amdgcn_s_sleep(PSLEEP);
                }
                if (wgid == 0 && lane == 0) __hip_atomic_store((GAS u64*)(ws + WS_E), E + 1, __ATOMIC_RELAXED, __HIP_MEMORY_SCOPE_AGENT);
            }
        }
        if (ABL & 16) { STAMP_DUMP(); return; }
        wait_flag(&FL[F_PCNT], 8);
        wait_flag_hot(&FL[F_PUB], 8);
        STAMP(1);
        const int p = lane >> 1, half = lane & 1;
        unsigned ra = WS_S2 + ((unsigned)(g * 32 + i) * 32 + p) * 128;
        v4u a0, a1; v2u ml;
        {
            int sp = 0;
#pragma nounroll
            for (;;) {
                asm volatile("" : "+v"(ra) :: "memory");
                a0 = __builtin_amdgcn_raw_buffer_load_b128(rs, ra + half * 32, 0, 16);
                a1 = __builtin_amdgcn_raw_buffer_load_b128(rs, ra + half * 32 + 16, 0, 16);
                ml = __builtin_amdgcn_raw_buffer_load_b64(rs, ra + 64, 0, 16);
                const bool ok = a0.x != SENT && a0.y != SENT && a0.z != SENT && a0.w != SENT && a1.x != SENT && a1.y != SENT && a1.z != SENT && a1.w != SENT && ml.x != SENT && ml.y != SENT;
                if (__all(ok) || ++sp > SPIN_MAX) break;
            }
        }
        STAMP(2);
        const float m = __uint_as_float(ml.x);
        float M = m;
        M = fmaxf(M, dpp<XOR2>(M)); M = fmaxf(M, dpp<ROR4>(M)); M = fmaxf(M, dpp<XOR8>(M)); M = xmax16(M);
        const float wgt = __expf(m - M);
        float Ls = wgt * __uint_as_float(ml.y);
        Ls += dpp<XOR2>(Ls); Ls += dpp<ROR4>(Ls); Ls += dpp<XOR8>(Ls); Ls = xsum16(Ls);
        float ov[8] = {wgt * __uint_as_float(a0.x), wgt * __uint_as_float(a0.y), wgt * __uint_as_float(a0.z), wgt * __uint_as_float(a0.w),
                       wgt * __uint_as_float(a1.x), wgt * __uint_as_float(a1.y), wgt * __uint_as_float(a1.z), wgt * __uint_as_float(a1.w)};
#pragma unroll
        for (int k = 0; k < 4; ++k) { const float t1 = ov[k] + dpp<XOR8>(ov[k]), t2 = ov[k + 4] + dpp<XOR8>(ov[k + 4]); ov[k] = (lane & 8) ? t2 : t1; }
#pragma unroll
        for (int k = 0; k < 2; ++k) { const float t1 = ov[k] + dpp<XOR2>(ov[k]), t2 = ov[k + 2] + dpp<XOR2>(ov[k + 2]); ov[k] = (lane & 2) ? t2 : t1; }
        { const float t1 = ov[0] + xor4(ov[0]), t2 = ov[1] + xor4(ov[1]); ov[0] = (lane & 4) ? t2 : t1; }
        const float of = xsum16(ov[0]) / Ls;
        const int od = 8 * half + ((lane >> 3) & 1) * 4 + ((lane >> 1) & 1) * 2 + ((lane >> 2) & 1), ob = lane >> 5;
        if ((lane & 16) == 0) *(LAS u16*)(L8 + L_OH + ob * 64 + ((od >> 2) * 8 + (od & 3)) * 2) = (u16)f2bf(of);
        flag_inc(flo + 4 * F_OH, lane);
        STAMP(3);
        if (i < 8) {
            const int rr = g * 8 + i, rb_ = rr >> 5, cb = rr & 31;
            const int pp = lane >> 2, q4 = lane & 3;
            const int ocol = 16 * cb + 4 * q4 + ((lane >> 3) & 1) * 2 + ((lane >> 2) & 1);
            const float xres = bf_lo((unsigned)x[rb_ * D + ocol]);
            wait_flag_hot(&FL[F_PUB3], 8);
            unsigned sa = WS_S3 + (unsigned)pp * 4096 + rb_ * 2048 + cb * 64 + q4 * 16;
            v4u t[16];
            int sp = 0;
#pragma nounroll
            for (;;) {
                bool ok = true;
                asm volatile("" : "+v"(sa) :: "memory");
#pragma unroll
                for (int j = 0; j < 16; ++j) t[j] = __builtin_amdgcn_raw_buffer_load_b128(rs, sa + j * 65536, 0, 16);
#pragma unroll
                for (int j = 0; j < 16; ++j) ok &= t[j].x != SENT && t[j].y != SENT && t[j].z != SENT && t[j].w != SENT;
                if (__all(ok) || ++sp > SPIN_MAX) break;
            }
            STAMP(4);
            float sv[4] = {0.f, 0.f, 0.f, 0.f};
#pragma unroll
            for (int j = 0; j < 16; ++j) { sv[0] += __uint_as_float(t[j].x); sv[1] += __uint_as_float(t[j].y); sv[2] += __uint_as_float(t[j].z); sv[3] += __uint_as_float(t[j].w); }
#pragma unroll
            for (int k = 0; k < 2; ++k) { const float t1 = sv[k] + dpp<XOR8>(sv[k]), t2 = sv[k + 2] + dpp<XOR8>(sv[k + 2]); sv[k] = (lane & 8) ? t2 : t1; }
            { const float t1 = sv[0] + xor4(sv[0]), t2 = sv[1] + xor4(sv[1]); sv[0] = (lane & 4) ? t2 : t1; }
            const float tot = xsum32(xsum16(sv[0]));
            if (lane < 16) out[rb_ * D + ocol] = (u16)f2bf(xres + tot);
            STAMP(5);
        }
    }
    STAMP_DUMP();
}

extern "C" void kernel_launch(void* const* d_in, const int* in_sizes, int n_in,
                              void* d_out, int out_size, void* d_ws, size_t ws_size,
                              hipStream_t stream) {
    fused_decode_kernel<<<NWG, NTHR, 0, stream>>>((const u16*)d_in[0], (const u16*)d_in[1], (const u16*)d_in[2], (const u16*)d_in[3],
                                                  (const u16*)d_in[4], (const u16*)d_in[6], (const u16*)d_in[7], (const u16*)d_in[8],
                                                  (u16*)d_out, (unsigned char*)d_ws);
}
```

```cpp
#include <hip/hip_runtime.h>
#include <hip/hip_bf16.h>

#define GAS __attribute__((address_space(1)))
#define LAS __attribute__((address_space(3)))
typedef unsigned v4u __attribute__((ext_vector_type(4)));
typedef unsigned v2u __attribute__((ext_vector_type(2)));
typedef float f4 __attribute__((ext_vector_type(4)));
typedef float f2 __attribute__((ext_vector_type(2)));
typedef short v4s __attribute__((ext_vector_type(4)));
typedef __bf16 v8bf __attribute__((ext_vector_type(8)));
typedef unsigned long long u64;
typedef unsigned short u16;

constexpr int INFL = 3;
constexpr int NV0 = 4;
constexpr int PSLEEP = 4;
constexpr int QDELAY = 56;
constexpr int ODELAY = 8;
constexpr int CODEKB = 14;
constexpr int NOFFN = 4;

constexpr int NWG = 256, NTHR = 704;
constexpr int D = 512, SK = 32768, DH = 128, WIN = 4096, FF = 1792, NQ = 4096;
constexpr float SCALE = 0.08838834764831845f;
constexpr unsigned SENT = 0xFFFFFFFFu;
constexpr int SPIN_MAX = 1 << 18;

constexpr unsigned WS_E = 0, WS_RR = 4096, WS_QP = 0x40000, WS_S2 = 0x80000, WS_S3 = 0x140000, WS_GU = 0x180000, WS_END = WS_GU + 0x80000;
constexpr int L_K = 0, L_V = 65536, L_W = 131072, L_XN = 147456, L_QS = L_XN + 2048, L_PF = L_QS + 1024, L_ML = L_PF + 8192,
              L_WT = L_ML + 256, L_OH = L_WT + 128, L_FLAG = L_OH + 384, L_X = L_FLAG + 64, L_EP = L_X + 2048, L_END = L_EP + 16;
constexpr int L_W2 = L_K;
constexpr int L_GU = L_K + 6 * 8192;
enum { F_LANDED = 0, F_PREFILL, F_GEMV, F_Q, F_KREAD, F_PCNT, F_PUB, F_OH, F_PUB3, F_X, F_XN };
constexpr int C_V0 = 4, C_K = C_V0 + NV0, C_V1 = C_K + 8, C_W2 = 20, C_END = 23;
constexpr int vpos(int c) { return c < NV0 ? C_V0 + c : C_V1 + (c - NV0); }

__device__ __forceinline__ float bf_lo(unsigned u) { return __builtin_bit_cast(float, u << 16); }
__device__ __forceinline__ float bf_hi(unsigned u) { return __builtin_bit_cast(float, u & 0xffff0000u); }
__device__ __forceinline__ unsigned f2bf(float f) { return (unsigned)__builtin_bit_cast(u16, (__bf16)f); }
__device__ __forceinline__ unsigned pk2(float lo, float hi) { return f2bf(lo) | (f2bf(hi) << 16); }
__device__ __forceinline__ float rbf(float f) { return (float)(__bf16)f; }
__device__ __forceinline__ unsigned canon(float f) { unsigned u = __builtin_bit_cast(unsigned, f); return (f != f) ? 0x7FC00000u : u; }
__device__ __forceinline__ v4u canon4(f4 v) { return v4u{canon(v[0]), canon(v[1]), canon(v[2]), canon(v[3])}; }
__device__ __forceinline__ bool nosent(v4u v) { return v.x != SENT && v.y != SENT && v.z != SENT && v.w != SENT; }
template <int CTRL> __device__ __forceinline__ float dpp(float x) {
    return __builtin_bit_cast(float, __builtin_amdgcn_mov_dpp(__builtin_bit_cast(int, x), CTRL, 0xf, 0xf, true));
}
constexpr int XOR1 = 0xB1, XOR2 = 0x4E, XOR7 = 0x141, XOR8 = 0x128;
__device__ __forceinline__ float xsum16(float x) { auto s = __builtin_amdgcn_permlane16_swap(__float_as_uint(x), __float_as_uint(x), false, false); return __uint_as_float(s[0]) + __uint_as_float(s[1]); }
__device__ __forceinline__ float xsum32(float x) { auto s = __builtin_amdgcn_permlane32_swap(__float_as_uint(x), __float_as_uint(x), false, false); return __uint_as_float(s[0]) + __uint_as_float(s[1]); }
__device__ __forceinline__ float xmax16(float x) { auto s = __builtin_amdgcn_permlane16_swap(__float_as_uint(x), __float_as_uint(x), false, false); return fmaxf(__uint_as_float(s[0]), __uint_as_float(s[1])); }
__device__ __forceinline__ float xmax32(float x) { auto s = __builtin_amdgcn_permlane32_swap(__float_as_uint(x), __float_as_uint(x), false, false); return fmaxf(__uint_as_float(s[0]), __uint_as_float(s[1])); }
__device__ __forceinline__ float row_sum(float x) { x += dpp<XOR1>(x); x += dpp<XOR2>(x); x += dpp<XOR7>(x); x += dpp<XOR8>(x); return x; }
__device__ __forceinline__ float row_max(float x) { x = fmaxf(x, dpp<XOR1>(x)); x = fmaxf(x, dpp<XOR2>(x)); x = fmaxf(x, dpp<XOR7>(x)); x = fmaxf(x, dpp<XOR8>(x)); return x; }
__device__ __forceinline__ f4 mfma(v4u a, v4u b, f4 c) {
    return __builtin_amdgcn_mfma_f32_16x16x32_bf16(__builtin_bit_cast(v8bf, a), __builtin_bit_cast(v8bf, b), c, 0, 0, 0);
}
__device__ __forceinline__ v4u trfrag(LAS unsigned char* a0, LAS unsigned char* a1) {
    const v4s x = __builtin_amdgcn_ds_read_tr16_b64_v4i16((LAS v4s*)a0), y = __builtin_amdgcn_ds_read_tr16_b64_v4i16((LAS v4s*)a1);
    return __builtin_bit_cast(v4u, __builtin_shufflevector(x, y, 0, 1, 2, 3, 4, 5, 6, 7));
}
#define LGKM0() asm volatile("s_waitcnt lgkmcnt(0)" ::: "memory")
#define VM0() asm volatile("s_waitcnt vmcnt(0)" ::: "memory")
#define MEMF() asm volatile("" ::: "memory")
#define BAR() do { __builtin_amdgcn_sched_barrier(0); asm volatile("s_barrier" ::: "memory"); __builtin_amdgcn_sched_barrier(0); } while (0)
#define GLDS(src, dst) __builtin_amdgcn_global_load_lds((const GAS unsigned*)(src), (LAS unsigned*)(dst), 16, 0, 2)
#define ST128(v, off) __builtin_amdgcn_raw_buffer_store_b128((v), rs, (off), 0, 16)
#define ST64(v, off) __builtin_amdgcn_raw_buffer_store_b64((v), rs, (off), 0, 16)
#define LD128(off) __builtin_amdgcn_raw_buffer_load_b128(rs, (off), 0, 16)
#define LD64(off) __builtin_amdgcn_raw_buffer_load_b64(rs, (off), 0, 16)

__device__ __forceinline__ void wait_flag(volatile LAS unsigned* f, unsigned v) {
    int sp = 0;
#pragma nounroll
    while (*f < v) { __builtin_amdgcn_s_sleep(1); if (++sp > SPIN_MAX) break; }
    MEMF();
}
__device__ __forceinline__ void wait_flag_hot(volatile LAS unsigned* f, unsigned v) {
    int sp = 0;
#pragma nounroll
    while (*f < v) { if (++sp > SPIN_MAX * 8) break; }
    MEMF();
}
__device__ __forceinline__ void flag_inc(int off, int lane) {
    if (lane == 0) asm volatile("ds_add_u32 %0, %1" ::"v"(off), "v"(1u) : "memory");
}
__device__ __forceinline__ v4u gran2(float a, float b, unsigned tl, unsigned th) {
    return v4u{((canon(a) + 0x80u) >> 8) | tl, th, ((canon(b) + 0x80u) >> 8) | tl, th};
}
__device__ __forceinline__ bool gran_ok(v4u v, unsigned tl, unsigned th) { return (((v.x ^ tl) | (v.z ^ tl)) >> 24) == 0u && v.y == th && v.w == th; }
__device__ __forceinline__ float gran_val(unsigned lo) { return __uint_as_float(lo << 8); }

__global__ void __launch_bounds__(NTHR, 3)
fused_decode_kernel(const u16* __restrict__ x, const u16* __restrict__ kc, const u16* __restrict__ vc, const u16* __restrict__ wq,
                    const u16* __restrict__ wo, const u16* __restrict__ wg, const u16* __restrict__ wu, const u16* __restrict__ wd,
                    u16* __restrict__ out, unsigned char* ws) {
    __shared__ __attribute__((aligned(16))) unsigned char lds[L_END];
    LAS unsigned char* L8 = (LAS unsigned char*)lds;
    volatile LAS unsigned* FL = (volatile LAS unsigned*)(L8 + L_FLAG);
    const int flo = (int)(unsigned)(size_t)(L8 + L_FLAG);
    const int tid = threadIdx.x, hw = __builtin_amdgcn_readfirstlane(tid >> 6), lane = tid & 63;
    const int wave = hw < 3 ? 10 - hw : hw - 3;
    const int wgid = blockIdx.x, g = wgid & 7, i = wgid >> 3, b = i >> 4, s = i & 15;
    const int lg = lane >> 4, lq = (lane >> 2) & 3, lp = lane & 3, l16 = lane & 15;
    const __amdgpu_buffer_rsrc_t rs = __builtin_amdgcn_make_buffer_rsrc(ws, 0, WS_END, 0x00020000);
    const int T = 4 * g + (i >> 3), tr = i & 7;
    const int rb = i >> 2, cq = i & 3;
    const bool ffn = g != NOFFN;
    const f4 z = {0.f, 0.f, 0.f, 0.f};
    const int r7 = (4 * lg + lq) & 7;
    u64 pc0;
    asm volatile("s_getpc_b64 %0" : "=s"(pc0));
    pc0 &= ~63ull;
    const int wsw = 8 * ((lane & 7) ^ (((lane >> 4) & 3) << 1));
    const int gc = g < NOFFN ? g : g - 1;
    const size_t fst = ffn ? 1 : 0;
    const size_t guo = fst * ((size_t)(64 * tr + (lane >> 3)) * FF + 64 * (4 * gc + (i >> 3)) + wsw);
    if (wave == 10) {
        if (lane == 0) __builtin_amdgcn_global_load_lds((const GAS unsigned*)(ws + WS_E), (LAS unsigned*)(L8 + L_EP), 16, 0, 0);
        GLDS(x + 8 * lane, L8 + L_X); GLDS(x + D + 8 * lane, L8 + L_X + 1024);
        const u16* src = wq + (size_t)(128 * (i >> 3) + (lane >> 3)) * NQ + 512 * g + 64 * tr + wsw;
#pragma unroll
        for (int j = 0; j < 16; ++j) GLDS(src + (size_t)j * 8 * NQ, L8 + L_W + j * 1024);
#pragma unroll
        for (int j = 0; j < 8; ++j) GLDS(wg + guo + fst * j * 8 * FF, L8 + L_GU + j * 1024);
    }
    if (wave == 0) { *(LAS unsigned*)(L8 + L_OH + lane * 4) = 0u; if (lane < 48) *(LAS unsigned*)(L8 + L_OH + 256 + lane * 4) = 0u; LGKM0(); }
    BAR();

    if (wave == 10) {
#define PACE(c_) do { if ((c_) >= INFL) { asm volatile("s_waitcnt vmcnt(%0)" ::"i"(8 * INFL) : "memory"); FL[F_LANDED] = (c_) - INFL + 1; } } while (0)
        asm volatile("s_waitcnt vmcnt(24)" ::: "memory");
        FL[F_X] = 1u;
        PACE(2);
#pragma unroll
        for (int j = 0; j < 8; ++j) GLDS(wu + guo + fst * j * 8 * FF, L8 + L_GU + 8192 + j * 1024);
        PACE(3);
        const size_t base = ((size_t)(b * 8 + g) * SK + (SK - WIN) + s * 256) * DH + lg * 128;
        const u16* kb = kc + base; const u16* vb = vc + base;
#pragma nounroll
        for (int c = 0; c < 16; ++c) {
            if (c >= NV0 && c < NV0 + 8) {
                const int kcn = c - NV0;
                if (kcn == 6 && FL[F_GEMV] < 1u) { VM0(); FL[F_LANDED] = C_V0 + c; wait_flag(&FL[F_GEMV], 1); }
#pragma unroll
                for (int j = 0; j < 8; ++j) GLDS(kb + kcn * 4096 + j * 512 + 8 * (l16 ^ ((4 * j + lg) & 15)), L8 + L_K + kcn * 8192 + j * 1024);
            } else {
                const int vcn = c < NV0 ? c : c - 8;
#pragma unroll
                for (int j = 0; j < 8; ++j) GLDS(vb + vcn * 4096 + j * 512 + 8 * (l16 ^ (((4 * j + lg) & 7) << 1)), L8 + L_V + vcn * 8192 + j * 1024);
            }
            PACE(C_V0 + c);
        }
        if (FL[F_KREAD] < 3u) { VM0(); FL[F_LANDED] = C_W2; wait_flag(&FL[F_KREAD], 3); }
#pragma nounroll
        for (int c = 0; c < 3; ++c) {
            const size_t st = c == 0 ? fst : 1;
            const u16* src = (c == 0 ? wd + st * (32 * (8 * gc + rb)) * D : wo + (size_t)(512 * g + 64 * rb + 32 * (c - 1)) * D) + st * (lg * D + 128 * cq);
            const int dst = L_W2 + (c == 0 ? 16384 : (c - 1) * 8192);
#pragma unroll
            for (int j = 0; j < 8; ++j) GLDS(src + st * (j * 4 * D + 8 * (l16 ^ (((4 * j + lg) & 7) << 1))), L8 + dst + j * 1024);
            PACE(C_W2 + c);
        }
#pragma unroll
        for (int k = INFL - 1; k >= 0; --k) {
            asm volatile("s_waitcnt vmcnt(%0)" ::"i"(8 * k) : "memory");
            FL[F_LANDED] = C_END - k;
        }
        return;
    }

    if (wave < 8) {
        const int w = wave;
        const v4u sent4 = {SENT, SENT, SENT, SENT};
        if (w < 3) ST128(sent4, WS_S2 + (unsigned)(g * 8 + rb) * 12288 + cq * 3072 + w * 1024 + lane * 16);
        if (i < 4) ST128(sent4, WS_S3 + (unsigned)(g * 4 + i) * 8192 + w * 1024 + lane * 16);
        VM0();
        flag_inc(flo + 4 * F_PREFILL, lane);
        wait_flag(&FL[F_LANDED], C_K + w + 1);
        const int kb = L_K + (32 * w + l16) * 256 + 16 * (lg ^ l16);
        v4u kf[8];
#pragma unroll
        for (int t = 0; t < 4; ++t) { kf[t] = *(const LAS v4u*)(L8 + (kb ^ (64 * t))); kf[4 + t] = *(const LAS v4u*)(L8 + (kb ^ (64 * t)) + 4096); }
        LGKM0();
        if (w < 3) flag_inc(flo + 4 * F_KREAD, lane);
        wait_flag(&FL[F_Q], 1);
        v4u qf[4];
#pragma unroll
        for (int t = 0; t < 4; ++t) qf[t] = *(const LAS v4u*)(L8 + L_QS + lp * 256 + t * 64 + lg * 16);
        f4 sa = z, sb = z;
#pragma unroll
        for (int t = 0; t < 4; ++t) { sa = mfma(kf[t], qf[t], sa); sb = mfma(kf[4 + t], qf[t], sb); }
        float sc[8];
#pragma unroll
        for (int k = 0; k < 4; ++k) { sc[k] = rbf(rbf(sa[k]) * SCALE); sc[4 + k] = rbf(rbf(sb[k]) * SCALE); }
        float m = fmaxf(fmaxf(fmaxf(sc[0], sc[1]), fmaxf(sc[2], sc[3])), fmaxf(fmaxf(sc[4], sc[5]), fmaxf(sc[6], sc[7])));
        m = xmax16(m); m = xmax32(m);
        float l = 0.f;
#pragma unroll
        for (int k = 0; k < 8; ++k) { sc[k] = __expf(sc[k] - m); l += sc[k]; }
        *(LAS v4u*)(L8 + L_PF + w * 1024 + lane * 16) = v4u{pk2(sc[0], sc[1]), pk2(sc[2], sc[3]), pk2(sc[4], sc[5]), pk2(sc[6], sc[7])};
        l = xsum16(l); l = xsum32(l);
        if (lane < 4) *(LAS f2*)(L8 + L_ML + lane * 64 + w * 8) = f2{m, l};
        flag_inc(flo + 4 * F_PCNT, lane);
        if (w < 4) {
            wait_flag_hot(&FL[F_PCNT], 8);
            v4u pf[8]; f4 ml[4];
#pragma unroll
            for (int c = 0; c < 8; ++c) pf[c] = *(const LAS v4u*)(L8 + L_PF + c * 1024 + lane * 16);
#pragma unroll
            for (int c = 0; c < 4; ++c) ml[c] = *(const LAS f4*)(L8 + L_ML + lp * 64 + c * 16);
            const float M = fmaxf(fmaxf(fmaxf(ml[0][0], ml[0][2]), fmaxf(ml[1][0], ml[1][2])), fmaxf(fmaxf(ml[2][0], ml[2][2]), fmaxf(ml[3][0], ml[3][2])));
            float al[8], Ls = 0.f;
#pragma unroll
            for (int c = 0; c < 8; ++c) { al[c] = __expf(ml[c >> 1][2 * (c & 1)] - M); Ls = fmaf(al[c], ml[c >> 1][2 * (c & 1) + 1], Ls); }
            LAS unsigned char* va = L8 + L_V + (4 * lg + lq) * 256 + 16 * ((4 * w + (lp >> 1)) ^ (r7 << 1)) + 8 * (lp & 1);
            LAS unsigned char* vb2 = L8 + L_V + (4 * lg + lq) * 256 + 16 * ((4 * w + 2 + (lp >> 1)) ^ (r7 << 1)) + 8 * (lp & 1);
            f4 acc0 = z, acc1 = z;
#pragma unroll
            for (int c = 0; c < 8; ++c) {
                v4u v0, v1;
                if (c < NV0) { v0 = trfrag(va + c * 8192, va + c * 8192 + 4096); v1 = trfrag(vb2 + c * 8192, vb2 + c * 8192 + 4096); }
                else {
                    int sp = 0;
#pragma nounroll
                    for (;;) {
                        const unsigned f = FL[F_LANDED];
                        MEMF();
                        v0 = trfrag(va + c * 8192, va + c * 8192 + 4096); v1 = trfrag(vb2 + c * 8192, vb2 + c * 8192 + 4096);
                        MEMF();
                        if (f >= (unsigned)(vpos(c) + 1) || ++sp > SPIN_MAX * 8) break;
                    }
                }
                acc0 += al[c] * mfma(v0, pf[c], z);
                acc1 += al[c] * mfma(v1, pf[c], z);
            }
            const bool mls = lane >= 8 && lane < 12 && (w & 1) == 0;
            if (l16 < 8 || mls) ST128(mls ? (v4u{canon(M), canon(Ls), 0u, 0u}) : canon4((l16 & 4) ? acc1 : acc0),
                                      WS_S2 + ((unsigned)(g * 8 + 2 * lp + (w >> 1)) * 32 + i) * 384 + (mls ? 256 : (w & 1) * 128 + (l16 >> 2) * 64 + lg * 16));
            flag_inc(flo + 4 * F_PUB, lane);
        } else {
            const int u = w - 4;
            wait_flag(&FL[F_LANDED], C_END);
            v4u wf[6];
            const int hoff = ffn ? 16384 : 0;
#pragma unroll
            for (int t = 0; t < 2; ++t) {
                LAS unsigned char* a = L8 + L_W2 + (4 * lg + lq) * 256 + 16 * ((4 * u + 2 * t + (lp >> 1)) ^ (r7 << 1)) + 8 * (lp & 1);
                wf[3 * t] = trfrag(a, a + 4096); wf[3 * t + 1] = trfrag(a + 8192, a + 12288); wf[3 * t + 2] = trfrag(a + hoff, a + hoff + 4096);
            }
            v4u o0, o1, o2;
            {
                int sp = 0;
#pragma nounroll
                for (;;) {
                    const unsigned f = FL[F_OH];
                    MEMF();
                    LAS unsigned char* a = L8 + L_OH + (lane & 1) * 192 + lg * 16;
                    o0 = *(const LAS v4u*)a; o1 = *(const LAS v4u*)(a + 64); o2 = *(const LAS v4u*)(a + 128);
                    MEMF();
                    if (f >= 2u || ++sp > SPIN_MAX * 8) break;
                }
            }
            const f4 d0 = mfma(wf[0], o0, mfma(wf[1], o1, mfma(wf[2], o2, z)));
            const f4 d1 = mfma(wf[3], o0, mfma(wf[4], o1, mfma(wf[5], o2, z)));
            if (l16 < 4) ST128(canon4((l16 & 2) ? d1 : d0), WS_S3 + ((unsigned)((lane & 1) * 16 + 4 * cq + u) * 64 + g * 8 + rb) * 128 + (l16 >> 1) * 64 + lg * 16);
            flag_inc(flo + 4 * F_PUB3, lane);
        }
        return;
    }

    const int pass = wave == 8;
    if (pass && i < 2) {
        unsigned acc = 0;
#pragma unroll
        for (int j = 0; j < CODEKB; ++j) acc ^= ((const GAS v4u*)pc0)[lane + 64 * j].x;
        asm volatile("" ::"v"(acc));
    }
    if (!pass) {
        wait_flag_hot(&FL[F_X], 1);
        const v4u xa = *(const LAS v4u*)(L8 + L_X + lane * 32), xb = *(const LAS v4u*)(L8 + L_X + lane * 32 + 16);
        float f[16] = {bf_lo(xa.x), bf_hi(xa.x), bf_lo(xa.y), bf_hi(xa.y), bf_lo(xa.z), bf_hi(xa.z), bf_lo(xa.w), bf_hi(xa.w),
                       bf_lo(xb.x), bf_hi(xb.x), bf_lo(xb.y), bf_hi(xb.y), bf_lo(xb.z), bf_hi(xb.z), bf_lo(xb.w), bf_hi(xb.w)};
        const float sm = (((f[0] + f[1]) + (f[2] + f[3])) + ((f[4] + f[5]) + (f[6] + f[7]))) + (((f[8] + f[9]) + (f[10] + f[11])) + ((f[12] + f[13]) + (f[14] + f[15])));
        const float mean = xsum16(row_sum(sm)) * (1.f / D);
        float q4[4] = {0.f, 0.f, 0.f, 0.f};
#pragma unroll
        for (int k = 0; k < 16; ++k) { const float d = f[k] - mean; q4[k & 3] = fmaf(d, d, q4[k & 3]); }
        const float var = rbf(xsum16(row_sum((q4[0] + q4[1]) + (q4[2] + q4[3]))) * (1.f / D));
        const float mb = rbf(mean), rden = __builtin_amdgcn_rcpf(rbf(__builtin_amdgcn_sqrtf(rbf(var + 1e-5f))));
#pragma unroll
        for (int k = 0; k < 16; ++k) f[k] = rbf(f[k] - mb) * rden;
        LAS unsigned char* xp = L8 + L_XN + (lane >> 5) * 1024 + (((lane & 31) >> 1) * 32 + (lane & 1) * 4) * 2;
#pragma unroll
        for (int gg = 0; gg < 4; ++gg) *(LAS v2u*)(xp + gg * 16) = v2u{pk2(f[4 * gg], f[4 * gg + 1]), pk2(f[4 * gg + 2], f[4 * gg + 3])};
        MEMF();
        FL[F_XN] = 1u;
    } else wait_flag(&FL[F_XN], 1);
    const v2u ep = *(const LAS v2u*)(L8 + L_EP);
    const u64 tag = (((u64)ep.y << 32) | ep.x) + 1;
    const unsigned tl = (unsigned)(tag << 24), th = (unsigned)(tag >> 8), tg0 = (unsigned)tag, tg1 = (unsigned)(tag >> 32);
    {
        const int wswz = ((4 * lg + lq) >> 1) & 3;
        const int sel = (l16 >> 1) & 3;
        wait_flag_hot(&FL[F_LANDED], pass ? 4u : 2u);
        LAS unsigned char* wa = L8 + (pass ? L_GU : L_W) + (4 * lg + lq) * 128 + 16 * (lp >> 1) + 8 * (lp & 1);
        LAS unsigned char* xa = L8 + L_XN + (lane & 1) * 1024 + lg * 16 + (pass ? 2 * tr : 4 * (i >> 3)) * 64;
        const int xh = pass ? 0 : 128;
        const v4u xf0 = *(const LAS v4u*)xa, xf1 = *(const LAS v4u*)(xa + 64), xf2 = *(const LAS v4u*)(xa + xh), xf3 = *(const LAS v4u*)(xa + xh + 64);
        f4 ac[8];
#pragma unroll
        for (int nt = 0; nt < 4; ++nt) {
            LAS unsigned char* a = wa + 32 * (nt ^ wswz);
            ac[nt] = mfma(trfrag(a, a + 2048), xf0, z);
            ac[nt] = mfma(trfrag(a + 4096, a + 6144), xf1, ac[nt]);
            ac[4 + nt] = mfma(trfrag(a + 8192, a + 10240), xf2, z);
            ac[4 + nt] = mfma(trfrag(a + 12288, a + 14336), xf3, ac[4 + nt]);
        }
        const f4 lo = sel == 0 ? ac[0] : sel == 1 ? ac[1] : sel == 2 ? ac[2] : ac[3];
        const f4 hi = sel == 0 ? ac[4] : sel == 1 ? ac[5] : sel == 2 ? ac[6] : ac[7];
        const unsigned co = (unsigned)(16 * sel + 4 * lg + 2 * (l16 >> 3)) * 8;
        const f4 r = pass ? lo : lo + hi;
        const v4u g0 = (l16 & 8) ? gran2(r[2], r[3], tl, th) : gran2(r[0], r[1], tl, th);
        if (!pass) {
            wait_flag_hot(&FL[F_PREFILL], 8u);
            ST128(g0, WS_QP + (((unsigned)(g * 2 + (lane & 1)) * 4 + (i >> 3)) * 512 + 64 * tr) * 8 + co);
            if (i < 4 && lane == 0) ST64((v2u{tg0, tg1}), WS_RR + (g * 4 + i) * 8);
        } else {
            FL[F_GEMV] = 1u;
            if (ffn) {
                const unsigned o = WS_GU + (((unsigned)(T * 8 + tr) * 2 + (lane & 1)) * 2) * 512 + co;
                ST128(g0, o);
                ST128((l16 & 8) ? gran2(hi[2], hi[3], tl, th) : gran2(hi[0], hi[1], tl, th), o + 512);
            }
        }
    }

    if (wave == 9) {
        __builtin_amdgcn_s_sleep(QDELAY);
        {
            unsigned qa = WS_QP + (unsigned)(g * 2 + b) * 16384 + lane * 16;
            v4u t[16];
            int sp = 0;
#pragma nounroll
            for (;;) {
                bool ok = true;
                asm volatile("" : "+v"(qa) :: "memory");
#pragma unroll
                for (int j = 0; j < 16; ++j) t[j] = LD128(qa + j * 1024);
#pragma unroll
                for (int j = 0; j < 16; ++j) ok &= gran_ok(t[j], tl, th);
                if (__all(ok) || ++sp > SPIN_MAX) break;
                __builtin_amdgcn_s_sleep(PSLEEP);
            }
#pragma unroll
            for (int h = 0; h < 4; ++h) {
                const float q0 = (gran_val(t[h].x) + gran_val(t[4 + h].x)) + (gran_val(t[8 + h].x) + gran_val(t[12 + h].x));
                const float q1 = (gran_val(t[h].z) + gran_val(t[4 + h].z)) + (gran_val(t[8 + h].z) + gran_val(t[12 + h].z));
                *(LAS unsigned*)(L8 + L_QS + h * 256 + lane * 4) = pk2(q0, q1);
            }
            MEMF();
            FL[F_Q] = 1u;
        }
        wait_flag(&FL[F_LANDED], C_W2);
        {
            unsigned ga = WS_GU + ((unsigned)((4 * g + (rb >> 1)) * 8) * 4 + lg) * 512 + (rb & 1) * 256 + l16 * 16;
            v4u t[8];
            int sp = 0;
#pragma nounroll
            for (;;) {
                asm volatile("" : "+v"(ga) :: "memory");
                const v2u rr = LD64(WS_RR + (lane & 31) * 8 + (ga & 1));
                bool ok = true;
                if (ffn) {
#pragma unroll
                    for (int j = 0; j < 8; ++j) t[j] = LD128(ga + j * 2048);
#pragma unroll
                    for (int j = 0; j < 8; ++j) ok &= gran_ok(t[j], tl, th);
                }
                ok &= rr.x == tg0 && rr.y == tg1;
                if (__all(ok) || ++sp > SPIN_MAX) break;
                __builtin_amdgcn_s_sleep(PSLEEP);
            }
            if (ffn) {
                float v0 = 0.f, v1 = 0.f;
#pragma unroll
                for (int j = 0; j < 8; ++j) { v0 += gran_val(t[j].x); v1 += gran_val(t[j].z); }
                v0 = rbf(v0); v1 = rbf(v1);
                auto s0 = __builtin_amdgcn_permlane16_swap(__float_as_uint(v0), __float_as_uint(v0), false, false);
                auto s1 = __builtin_amdgcn_permlane16_swap(__float_as_uint(v1), __float_as_uint(v1), false, false);
                const float u0 = __uint_as_float(s0[1]), u1 = __uint_as_float(s1[1]);
                const float h0 = rbf(v0 / (1.f + __expf(-v0))) * u0, h1 = rbf(v1 / (1.f + __expf(-v1))) * u1;
                const int k = 2 * l16;
                if ((lg & 1) == 0) *(LAS unsigned*)(L8 + L_OH + (lg >> 1) * 192 + (64 + 8 * ((k >> 2) & 3) + 4 * (k >> 4) + (k & 3)) * 2) = pk2(h0, h1);
            }
        }
        MEMF();
        flag_inc(flo + 4 * F_OH, lane);
        return;
    }

    wait_flag(&FL[F_PCNT], 8);
    wait_flag_hot(&FL[F_PUB], 4);
    wait_flag_hot(&FL[F_LANDED], C_END);
    __builtin_amdgcn_s_sleep(ODELAY);
    {
        unsigned ra = WS_S2 + (unsigned)(g * 8 + rb) * 12288 + lg * 384 + l16 * 16;
        const unsigned ma = WS_S2 + (unsigned)(g * 8 + rb) * 12288 + (lane & 31) * 384 + 256;
        v4u t[8]; v2u ml;
        int sp = 0;
#pragma nounroll
        for (;;) {
            asm volatile("" : "+v"(ra) :: "memory");
            ml = LD64(ma + (ra & 1));
#pragma unroll
            for (int j = 0; j < 8; ++j) t[j] = LD128(ra + j * 1536);
            bool ok = ml.x != SENT && ml.y != SENT;
#pragma unroll
            for (int j = 0; j < 8; ++j) ok &= nosent(t[j]);
            if (__all(ok) || ++sp > SPIN_MAX) break;
        }
        float wt[8];
        {
            const float m = __uint_as_float(ml.x);
            const float M = row_max(m);
            const float e = __expf(m - M);
            const float Ls = row_sum(e * __uint_as_float(ml.y));
            if (lane < 32) *(LAS float*)(L8 + L_WT + ((lane & 3) * 8 + (lane >> 2)) * 4) = e / Ls;
            const f4 w0 = *(const LAS f4*)(L8 + L_WT + lg * 32), w1 = *(const LAS f4*)(L8 + L_WT + lg * 32 + 16);
#pragma unroll
            for (int j = 0; j < 4; ++j) { wt[j] = w0[j]; wt[4 + j] = w1[j]; }
        }
        f4 oa = z, ob = z;
#pragma unroll
        for (int j = 0; j < 4; ++j) { oa += wt[j] * __builtin_bit_cast(f4, t[j]); ob += wt[4 + j] * __builtin_bit_cast(f4, t[4 + j]); }
        float o4[4];
#pragma unroll
        for (int k = 0; k < 4; ++k) {
            auto sw = __builtin_amdgcn_permlane32_swap(__float_as_uint(oa[k]), __float_as_uint(ob[k]), false, false);
            o4[k] = xsum16(__uint_as_float(sw[0]) + __uint_as_float(sw[1]));
        }
        if ((lg & 1) == 0) *(LAS v2u*)(L8 + L_OH + (lg >> 1) * 192 + (32 * (l16 >> 3) + 8 * (l16 & 3) + 4 * ((l16 >> 2) & 1)) * 2) = v2u{pk2(o4[0], o4[1]), pk2(o4[2], o4[3])};
    }
    flag_inc(flo + 4 * F_OH, lane);
    if (i < 4) {
        const int rr = g * 4 + i, ocol = (rr >> 4) * D + 32 * (rr & 15) + 4 * (lane & 7);
        const v2u xr = *(const LAS v2u*)(L8 + L_X + ocol * 2);
        wait_flag_hot(&FL[F_PUB3], 4);
        unsigned sa = WS_S3 + (unsigned)rr * 8192 + lane * 16;
        v4u t[8];
        int sp = 0;
#pragma nounroll
        for (;;) {
            bool ok = true;
            asm volatile("" : "+v"(sa) :: "memory");
#pragma unroll
            for (int j = 0; j < 8; ++j) t[j] = LD128(sa + j * 1024);
#pragma unroll
            for (int j = 0; j < 8; ++j) ok &= nosent(t[j]);
            if (__all(ok) || ++sp > SPIN_MAX) break;
        }
        f4 sv = z;
#pragma unroll
        for (int j = 0; j < 8; ++j) sv += __builtin_bit_cast(f4, t[j]);
#pragma unroll
        for (int k = 0; k < 4; ++k) { float v = sv[k]; v += dpp<XOR8>(v); sv[k] = xsum32(xsum16(v)); }
        if (lane < 8) *(GAS v2u*)(out + ocol) = v2u{pk2(bf_lo(xr.x) + sv[0], bf_hi(xr.x) + sv[1]), pk2(bf_lo(xr.y) + sv[2], bf_hi(xr.y) + sv[3])};
        if (wgid == 0 && lane == 0) ST64((v2u{tg0, tg1}), WS_E);
    }
}

extern "C" void kernel_launch(void* const* d_in, const int* in_sizes, int n_in,
                              void* d_out, int out_size, void* d_ws, size_t ws_size,
                              hipStream_t stream) {
    fused_decode_kernel<<<NWG, NTHR, 0, stream>>>((const u16*)d_in[0], (const u16*)d_in[1], (const u16*)d_in[2], (const u16*)d_in[3],
                                                  (const u16*)d_in[4], (const u16*)d_in[6], (const u16*)d_in[7], (const u16*)d_in[8],
                                                  (u16*)d_out, (unsigned char*)d_ws);
}
```
